```python
import torch

def _softmax(x, axis=-1):
    return torch.softmax(x, dim=axis)
def _gelu(x):
    return torch.nn.functional.gelu(x, approximate="tanh")
def _silu(x):
    return torch.nn.functional.silu(x)
def _sigmoid(x):
    return torch.sigmoid(x)
def _layernorm(x, axis=-1, eps=1e-5):
    m = x.mean(dim=axis, keepdim=True)
    v = x.var(dim=axis, keepdim=True, unbiased=False)
    return (x - m) / torch.sqrt(v + eps)
def _scatter_add(tgt, idx, src, axis=0):
    return tgt.clone().index_add_(axis, idx, src)
def _segment_sum(src, seg, n):
    out = torch.zeros((n,) + src.shape[1:], dtype=src.dtype)
    return out.index_add_(0, seg, src)
def _pad(x, pw):
    flat = tuple(v for pair in reversed(pw) for v in pair)
    return torch.nn.functional.pad(x, flat)

kernel_name = 'gated_mix__sliding_block_cached__swiglu_ffn__decode'

def setup_inputs():

    _w = (lambda a: (lambda t: t.to(torch.bfloat16) if t.is_floating_point() else t)(torch.as_tensor(a)))
    def _A_setup():
        B, Sq, D = 2, 1, 512
        Hq, Hkv, Sk, Dh = 32, 8, 32768, 128
        assert Hq % Hkv == 0, (Hq, Hkv)
        return {
            "x": _w((lambda *s: torch.randn(*s, dtype=torch.bfloat16) * 0.05)(B, Sq, D)),
            "k_cache": _w((lambda *s: torch.randn(*s, dtype=torch.bfloat16) * 6.4)(B, Hkv, Sk, Dh)),
            "v_cache": _w((lambda *s: torch.randn(*s, dtype=torch.bfloat16) * 0.2)(B, Hkv, Sk, Dh)),
            "wq": _w((lambda *s: torch.randn(*s, dtype=torch.bfloat16) * 6.4)(D, Hq * Dh) * (D ** -0.5)),
            "wo": _w((lambda *s: torch.randn(*s, dtype=torch.bfloat16) * 0.2)(Hq * Dh, D) * ((Hq * Dh) ** -0.5)),
        }
    _A_in = _A_setup()

    def _B_setup():
        B, S, D, F = 2, 1, 512, 1792
        return {
            "x": _w((lambda *s: torch.randn(*s, dtype=torch.bfloat16) * 0.05)(B, S, D)),
            "w_gate": _w((lambda *s: torch.randn(*s, dtype=torch.bfloat16) * 0.05)(D, F)),
            "w_up": _w((lambda *s: torch.randn(*s, dtype=torch.bfloat16) * 0.05)(D, F)),
            "w_down": _w((lambda *s: torch.randn(*s, dtype=torch.bfloat16) * 0.05)(F, D)),
        }
    _B_in = _B_setup()

    return {
        "A_x": _A_in["x"],
        "A_k_cache": _A_in["k_cache"],
        "A_v_cache": _A_in["v_cache"],
        "A_wq": _A_in["wq"],
        "A_wo": _A_in["wo"],
        "B_x": _B_in["x"],
        "B_w_gate": _B_in["w_gate"],
        "B_w_up": _B_in["w_up"],
        "B_w_down": _B_in["w_down"],
        "gate_w": _w((lambda *s: torch.randn(*s, dtype=torch.bfloat16) * 0.05)(512) * 16.0),
    }

def reference(A_x, A_k_cache, A_v_cache, A_wq, A_wo, B_x, B_w_gate, B_w_up, B_w_down, gate_w):
    def _A_ref(x, k_cache, v_cache, wq, wo):
        B, Sq, D = x.shape
        Hq, Dh = 32, 128
        Hkv, Sk = k_cache.shape[1], k_cache.shape[2]
        W = min(4096, max(256, Sk // 4))
        rep = Hq // Hkv
        q = (lambda x, *d: x.permute(*d))((x @ wq).reshape(B, Sq, Hq, Dh), 0, 2, 1, 3)
        k = (lambda x, n, axis: torch.repeat_interleave(x, n, dim=axis))(k_cache[:, :, Sk - W:, :], rep, axis=1)
        v = (lambda x, n, axis: torch.repeat_interleave(x, n, dim=axis))(v_cache[:, :, Sk - W:, :], rep, axis=1)
        scores = torch.einsum("bhqd,bhkd->bhqk", q, k) * (Dh ** -0.5)
        p = _softmax(scores, axis=-1)
        o = torch.einsum("bhqk,bhkd->bhqd", p, v)
        return (lambda x, *d: x.permute(*d))(o, 0, 2, 1, 3).reshape(B, Sq, Hq * Dh) @ wo

    def _B_ref(x, w_gate, w_up, w_down):
        g = _silu(x @ w_gate)
        u = x @ w_up
        return (g * u) @ w_down

    x = A_x
    g = _sigmoid((x * gate_w).sum(-1, keepdims=True))
    a = _A_ref(x, A_k_cache, A_v_cache, A_wq, A_wo)
    b = _B_ref(x, B_w_gate, B_w_up, B_w_down)
    return g * a + (1 - g) * b
```

```cpp
#include <hip/hip_runtime.h>
#include <hip/hip_bf16.h>
#include <stdint.h>

#define GAS __attribute__((address_space(1)))
#define LAS __attribute__((address_space(3)))
#define RLX_AGENT __ATOMIC_RELAXED, __HIP_MEMORY_SCOPE_AGENT
#define SB() __builtin_amdgcn_sched_barrier(0)
typedef unsigned v4u __attribute__((ext_vector_type(4)));
typedef unsigned v2u __attribute__((ext_vector_type(2)));
typedef float f32x4 __attribute__((ext_vector_type(4)));
typedef float f32x2 __attribute__((ext_vector_type(2)));
typedef __bf16 bf16x8 __attribute__((ext_vector_type(8)));
typedef __bf16 bf16x2 __attribute__((ext_vector_type(2)));
typedef short s16x4 __attribute__((ext_vector_type(4)));

namespace {

constexpr int SK = 32768, WIN = 4096;
constexpr int WS_EPOCH = 0;
constexpr int WS_Q = 4096;
constexpr int WS_ML = WS_Q + 8 * 2 * 4 * 128 * 16;
constexpr int WS_F = WS_ML + 8 * 4 * 32 * 16;
constexpr int WS_O = WS_F + 224 * 4096;
constexpr int WS_R = WS_O + 256 * 4096;
constexpr int WS_END = WS_R + 64 * 32768;
constexpr int WS_DIAG = WS_END;
constexpr int WS_RSRC_BYTES = WS_END;
constexpr int OOB = 0x7fffff00;

constexpr int L_FLAG = 0;
constexpr int L_ZERO = 192;
constexpr int L_QS = 256;
constexpr int L_P = L_QS + 1024;
constexpr int L_MX = L_P + 2048;
constexpr int L_LS = L_MX + 64;
constexpr int L_AV = L_LS + 64;
constexpr int L_GT = L_AV + 256;
constexpr int L_RED = L_GT + 16;
constexpr int L_OM = L_RED + 512;
constexpr int L_BYTES = L_OM + 8192;
constexpr int F_KISS = 0, F_Q = 4, F_P = 8, F_L = 12, F_O = 16, F_AV = 20, F_PUB = 24, F_G = 28;

constexpr unsigned long long TLIMIT = 20000000ull;

struct Params {
  const unsigned char *x, *k, *v, *wq, *wo, *wg, *wu, *wd, *gw;
  unsigned char *out, *ws;
};

typedef __amdgpu_buffer_rsrc_t rsrc_t;
__device__ __forceinline__ rsrc_t mkrs(const unsigned char* base, int bytes) {
  return __builtin_amdgcn_make_buffer_rsrc((void*)base, 0, bytes, 0x00020000);
}
__device__ __forceinline__ v4u ldnt(rsrc_t r, int voff, int soff) { return __builtin_amdgcn_raw_buffer_load_b128(r, voff, soff, 2); }
__device__ __forceinline__ unsigned pkbf(float lo, float hi) {
  bf16x2 r = {(__bf16)lo, (__bf16)hi};
  return __builtin_bit_cast(unsigned, r);
}
__device__ __forceinline__ float bflo(unsigned u) { return __builtin_bit_cast(float, u << 16); }
__device__ __forceinline__ float bfhi(unsigned u) { return __builtin_bit_cast(float, u & 0xffff0000u); }
__device__ __forceinline__ float u2f(unsigned u) { return __builtin_bit_cast(float, u); }
__device__ __forceinline__ unsigned f2u(float f) { return __builtin_bit_cast(unsigned, f); }
__device__ __forceinline__ f32x4 mfma32(v4u a, v4u b, f32x4 c) {
  return __builtin_amdgcn_mfma_f32_16x16x32_bf16(__builtin_bit_cast(bf16x8, a), __builtin_bit_cast(bf16x8, b), c, 0, 0, 0);
}
__device__ __forceinline__ f32x4 mfma16(unsigned a0, unsigned a1, unsigned b0, unsigned b1, f32x4 c) {
  v2u a = {a0, a1}, b = {b0, b1};
  return __builtin_amdgcn_mfma_f32_16x16x16bf16_1k(__builtin_bit_cast(s16x4, a), __builtin_bit_cast(s16x4, b), c, 0, 0, 0);
}
template <int CTRL> __device__ __forceinline__ float dppf(float x) {
  return u2f(__builtin_amdgcn_mov_dpp((int)f2u(x), CTRL, 0xf, 0xf, true));
}
template <int CTRL, int ROWMASK> __device__ __forceinline__ float dppf_rows(float x) {
  return u2f(__builtin_amdgcn_update_dpp((int)f2u(x), (int)f2u(x), CTRL, ROWMASK, 0xf, false));
}
constexpr int ROR8 = 0x128, ROR4 = 0x124, ROR2 = 0x122, ROR1 = 0x121;
__device__ __forceinline__ float swap32_add(float a, float b) {
  auto r = __builtin_amdgcn_permlane32_swap(f2u(a), f2u(b), false, false);
  return u2f(r[0]) + u2f(r[1]);
}
__device__ __forceinline__ float swap32_max(float a, float b) {
  auto r = __builtin_amdgcn_permlane32_swap(f2u(a), f2u(b), false, false);
  return fmaxf(u2f(r[0]), u2f(r[1]));
}
__device__ __forceinline__ float swap16_add(float a, float b) {
  auto r = __builtin_amdgcn_permlane16_swap(f2u(a), f2u(b), false, false);
  return u2f(r[0]) + u2f(r[1]);
}
__device__ __forceinline__ float swap16_max(float a, float b) {
  auto r = __builtin_amdgcn_permlane16_swap(f2u(a), f2u(b), false, false);
  return fmaxf(u2f(r[0]), u2f(r[1]));
}
__device__ __forceinline__ float xrow_max(float x) { x = swap16_max(x, x); return swap32_max(x, x); }
__device__ __forceinline__ float xrow_sum(float x) { x = swap16_add(x, x); return swap32_add(x, x); }
__device__ __forceinline__ float row_max(float x) {
  x = fmaxf(x, dppf<ROR8>(x)); x = fmaxf(x, dppf<ROR4>(x)); x = fmaxf(x, dppf<ROR2>(x)); return fmaxf(x, dppf<ROR1>(x));
}
__device__ __forceinline__ float row_sum(float x) {
  x += dppf<ROR8>(x); x += dppf<ROR4>(x); x += dppf<ROR2>(x); return x + dppf<ROR1>(x);
}
__device__ __forceinline__ float rdlane(float x, int l) { return u2f(__builtin_amdgcn_readlane(f2u(x), l)); }
#define LD_AUX 16
#define ST_AUX 16
__device__ __forceinline__ v4u ldu(rsrc_t rs, int off) { return __builtin_amdgcn_raw_buffer_load_b128(rs, off, 0, LD_AUX); }
__device__ __forceinline__ void stu(rsrc_t rs, int off, unsigned v0, unsigned v1, unsigned tag) {
  v4u u = {v0, tag, v1, tag};
  __builtin_amdgcn_raw_buffer_store_b128(u, rs, off, 0, ST_AUX);
}
__device__ __forceinline__ bool uok(v4u u, unsigned tag) { return u.y == tag && u.w == tag; }

__device__ __forceinline__ unsigned load_tag(const unsigned char* ws) {
  unsigned e;
  asm volatile("s_load_dword %0, %1, 0x0\n\ts_waitcnt lgkmcnt(0)" : "=s"(e) : "s"(ws) : "memory");
  SB();
  return e + 1u;
}

struct Spin {
  unsigned n = 0; unsigned long long t0 = 0;
  __device__ __forceinline__ bool expired() {
    if ((++n & 255u) != 0u) return false;
    const unsigned long long t = __builtin_amdgcn_s_memrealtime();
    if (t0 == 0) { t0 = t; return false; }
    return t - t0 > TLIMIT;
  }
};
template <int SLEEP> __device__ __forceinline__ void lds_wait4(LAS unsigned char* L, int grp) {
  Spin sp;
  for (;;) {
    const v4u f = *(volatile LAS v4u*)(L + L_FLAG + grp * 4);
    if (__builtin_amdgcn_readfirstlane(f.x & f.y & f.z & f.w) != 0u) break;
    if (sp.expired()) break;
    if (SLEEP) __builtin_amdgcn_s_sleep(SLEEP);
  }
  asm volatile("" ::: "memory");
}
template <int SLEEP> __device__ __forceinline__ void lds_wait1(LAS unsigned char* L, int word) {
  Spin sp;
  for (;;) {
    const unsigned f = *(volatile LAS unsigned*)(L + L_FLAG + word * 4);
    if (__builtin_amdgcn_readfirstlane(f) != 0u) break;
    if (sp.expired()) break;
    if (SLEEP) __builtin_amdgcn_s_sleep(SLEEP);
  }
  asm volatile("" ::: "memory");
}
__device__ __forceinline__ void lds_set(LAS unsigned char* L, int word) {
  asm volatile("" ::: "memory");
  *(volatile LAS unsigned*)(L + L_FLAG + word * 4) = 1u;
}
__device__ __forceinline__ void delta_sel(int jj, unsigned sb, unsigned& s0, unsigned& s1) {
  const unsigned pr = sb | ((sb + 1u) << 8);
  const unsigned lo = 0x0c0c0000u | pr, hi = (pr << 16) | 0x0c0cu, z = 0x0c0c0c0cu;
  s0 = jj == 0 ? lo : (jj == 1 ? hi : z);
  s1 = jj == 2 ? lo : (jj == 3 ? hi : z);
}
#define PERM(x, s) __builtin_amdgcn_perm(0u, (x), (s))

#define STAMP(k) do { } while (0)

__global__ void __launch_bounds__(512, 2) fused_kernel(Params p) {
  __shared__ __attribute__((aligned(16))) unsigned char lds_raw[L_BYTES];
  LAS unsigned char* L = (LAS unsigned char*)lds_raw;
  const int tid = threadIdx.x, lane = tid & 63, wave = __builtin_amdgcn_readfirstlane(tid >> 6);
  const int blk = blockIdx.x, xg = blk & 7, xj = blk >> 3;
  const int n16 = lane & 15, a4 = lane >> 4, w = wave & 3;
  const int c8 = lane & 7, r0 = (lane >> 3) & 1, rho = 2 * a4 + r0;
  const rsrc_t rs = mkrs(p.ws, WS_RSRC_BYTES);
  const int ab = xj >> 4, as = xj & 15;
  const size_t rbase = ((size_t)(ab * 8 + xg) * SK + (SK - WIN)) * 256;
  const int wofs = (4 * as + w) * 1024;
  const bool hasF = xj < 28;
  const int fc = xg * 28 + xj, oi = xg * 32 + xj;
  STAMP(0);
  unsigned sE0, sE1, sO0, sO1;
  delta_sel(n16 & 3, 0u, sE0, sE1);
  delta_sel(n16 & 3, 2u, sO0, sO1);
  unsigned tag;

  if (wave < 4) {
    const rsrc_t krs = mkrs(p.k + rbase, 1 << 20);
    const int ko0 = c8 * 65536 + a4 * 16 + r0 * 64, ko1 = c8 * 65536 + a4 * 16 + (r0 ^ 1) * 64 + 524288;
    const int qoff = WS_Q + (((xg * 2 + ab) * 4 + 2 * (lane >> 5)) * 128 + w * 32 + (lane & 31)) * 16;
    v4u kr[16];
    SB();
    __builtin_amdgcn_s_barrier();
    SB();
#pragma unroll
    for (int u = 0; u < 16; ++u) {
      kr[u] = ldnt(krs, ((u >> 3) ? ko1 : ko0) + ((u >> 1) & 3) * 256 + (u & 1) * 128, wofs);
      SB();
    }
    lds_set(L, F_KISS + w);
    STAMP(1);
    SB();
    tag = load_tag(p.ws);
    SB();
    STAMP(2);
    v4u qp[4][2];
#pragma unroll
    for (int r = 0; r < 4; ++r) {
      asm volatile("" :: "v"(kr[4 * r + 3]));
      SB();
      qp[r][0] = ldu(rs, qoff); qp[r][1] = ldu(rs, qoff + 2048);
      SB();
    }
    STAMP(3);
    v4u q0 = qp[0][0], q1 = qp[0][1];
    bool got = __all(uok(q0, tag) && uok(q1, tag));
#pragma unroll
    for (int r = 1; r < 4; ++r) {
      if (!got) { q0 = qp[r][0]; q1 = qp[r][1]; got = __all(uok(q0, tag) && uok(q1, tag)); }
    }
    if (!got) {
      Spin sp;
      for (;;) {
        asm volatile("" ::: "memory");
        q0 = ldu(rs, qoff); q1 = ldu(rs, qoff + 2048);
        if (__all(uok(q0, tag) && uok(q1, tag))) break;
        if (sp.expired()) break;
      }
    }
    STAMP(4);
    {
      constexpr float QS = 0.088388347648318440f * 1.4426950408889634f;
      const float c0 = bflo(q0.x) + bflo(q1.x), c1 = bfhi(q0.x) + bfhi(q1.x), c2 = bflo(q0.z) + bflo(q1.z), c3 = bfhi(q0.z) + bfhi(q1.z);
      const float e0 = swap32_add(c0, c2) * QS, e1 = swap32_add(c1, c3) * QS;
      const int pos = lane & 31;
      const int dim = (pos >> 4) * 64 + (pos & 7) * 8 + ((pos >> 3) & 1) * 4 + (lane >> 5) * 2;
      *(LAS unsigned*)(L + L_QS + (w * 128 + dim) * 2) = pkbf(e0, e1);
    }
    lds_set(L, F_Q + w);
    lds_wait4<0>(L, F_Q);
    const int qa = L_QS + ((n16 & 3) * 128 + ((n16 >> 2) & 1) * 32 + a4 * 8) * 2;
    const int qaa = n16 < 8 ? qa : L_ZERO - 128, qab = n16 < 8 ? L_ZERO - 128 : qa;
    const v4u qa0 = *(LAS v4u*)(L + (n16 < 8 ? qa : L_ZERO)), qa1 = *(LAS v4u*)(L + qaa + 128);
    const v4u qb0 = *(LAS v4u*)(L + (n16 < 8 ? L_ZERO : qa)), qb1 = *(LAS v4u*)(L + qab + 128);
    f32x4 acc[2][2];
#pragma unroll
    for (int e = 0; e < 2; ++e)
#pragma unroll
      for (int P = 0; P < 2; ++P) {
        f32x4 a = {0.f, 0.f, 0.f, 0.f};
        a = mfma32(qa0, kr[e * 8 + P * 2], a); a = mfma32(qa1, kr[e * 8 + P * 2 + 1], a);
        a = mfma32(qb0, kr[e * 8 + P * 2 + 4], a); a = mfma32(qb1, kr[e * 8 + P * 2 + 5], a);
        acc[e][P] = a;
      }
    const bool sel0 = ((a4 & 1) == (n16 >> 3));
    float S[4];
#pragma unroll
    for (int i = 0; i < 4; ++i) {
      const float y0 = dppf_rows<ROR8, 0xa>(sel0 ? acc[0][0][i] : acc[1][0][i]);
      const float y1 = dppf_rows<ROR8, 0xa>(sel0 ? acc[0][1][i] : acc[1][1][i]);
      S[i] = swap16_add(y0, y1);
    }
    const float mrow = row_max(swap16_max(swap32_max(S[0], S[1]), swap32_max(S[2], S[3])));
    const float m0 = rdlane(mrow, 0), m2 = rdlane(mrow, 16), m1 = rdlane(mrow, 32), m3 = rdlane(mrow, 48);
    const unsigned P01 = pkbf(__builtin_amdgcn_exp2f(S[0] - m0), __builtin_amdgcn_exp2f(S[1] - m1));
    const unsigned P23 = pkbf(__builtin_amdgcn_exp2f(S[2] - m2), __builtin_amdgcn_exp2f(S[3] - m3));
    {
      LAS unsigned short* pp = (LAS unsigned short*)(L + L_P + w * 512 + lane * 2);
      pp[0] = (unsigned short)P01; pp[64] = (unsigned short)(P01 >> 16); pp[128] = (unsigned short)P23; pp[192] = (unsigned short)(P23 >> 16);
    }
    lds_set(L, F_P + w);
    STAMP(5);
    {
      const float lrow = row_sum(swap16_add(swap32_add(bflo(P01), bfhi(P01)), swap32_add(bflo(P23), bfhi(P23))));
      const int hrow = ((a4 & 1) << 1) | (a4 >> 1);
      if (n16 == 0) { *(LAS float*)(L + L_MX + w * 16 + hrow * 4) = mrow; *(LAS float*)(L + L_LS + w * 16 + hrow * 4) = lrow; }
    }
    lds_set(L, F_L + w);
    lds_wait4<0>(L, F_L);
    {
      float mw[4], lw[4], M = -INFINITY, lsum = 0.f;
#pragma unroll
      for (int ww = 0; ww < 4; ++ww) { mw[ww] = *(LAS float*)(L + L_MX + ww * 16 + w * 4); lw[ww] = *(LAS float*)(L + L_LS + ww * 16 + w * 4); M = fmaxf(M, mw[ww]); }
#pragma unroll
      for (int ww = 0; ww < 4; ++ww) lsum = fmaf(__builtin_amdgcn_exp2f(mw[ww] - M), lw[ww], lsum);
      stu(rs, lane == 0 ? WS_ML + ((xg * 4 + w) * 32 + xj) * 16 : OOB, f2u(M), f2u(lsum), tag);
    }
#pragma unroll
    for (int r = 0; r < 4; ++r) asm volatile("" :: "v"(qp[r][0]), "v"(qp[r][1]));
    STAMP(6);
    lds_wait1<1>(L, F_G);
    if ((w & 1) == 0) {
      const int bb = w >> 1;
      const float gate = *(LAS float*)(L + L_GT + bb * 4);
      const int mloff = WS_ML + ((xg * 4 + (xj >> 3)) * 32 + bb * 16 + (lane >> 3)) * 16;
      v4u ml0, ml1;
      {
        Spin sp;
        for (;;) {
          asm volatile("" ::: "memory");
          ml0 = ldu(rs, mloff); ml1 = ldu(rs, mloff + 128);
          if (__all(uok(ml0, tag) && uok(ml1, tag))) break;
          if (sp.expired()) break;
        }
      }
      float mm = fmaxf(u2f(ml0.x), u2f(ml1.x));
      mm = fmaxf(mm, dppf<ROR8>(mm)); mm = xrow_max(mm);
      const float e0 = __builtin_amdgcn_exp2f(u2f(ml0.x) - mm), e1 = __builtin_amdgcn_exp2f(u2f(ml1.x) - mm);
      float ll = e0 * u2f(ml0.z) + e1 * u2f(ml1.z);
      ll += dppf<ROR8>(ll); ll = xrow_sum(ll);
      const float sc = gate / ll, w0 = e0 * sc, w1 = e1 * sc;
      STAMP(7);
      const int ooff = WS_O + ((oi * 32 + bb * 16) * 8 + lane) * 16;
      v4u u0, u1;
      {
        Spin sp;
        for (;;) {
          asm volatile("" ::: "memory");
          u0 = ldu(rs, ooff); u1 = ldu(rs, ooff + 1024);
          if (__all(uok(u0, tag) && uok(u1, tag))) break;
          if (sp.expired()) break;
        }
      }
      STAMP(8);
      const float v0 = w0 * u2f(u0.x) + w1 * u2f(u1.x), v1 = w0 * u2f(u0.z) + w1 * u2f(u1.z);
      float vv = swap32_add(v0, v1);
      vv = swap16_add(vv, vv); vv += dppf<ROR8>(vv);
      const unsigned hh = pkbf(vv, vv);
      const unsigned lo = pkbf(vv - bflo(hh), 0.f);
      const int r = 2 * c8 + (lane >> 5);
      if ((lane & 16) == 0) *(LAS unsigned short*)(L + L_AV + ((r0 * 2 + bb) * 4 + (r & 3)) * 16 + (r >> 2) * 2) = (unsigned short)(r0 ? lo : hh);
    } else if (w == 1) {
      float hv = 0.f;
      const float gate = *(LAS float*)(L + L_GT + (lane >> 5) * 4);
      if (hasF) {
        const int foff = WS_F + fc * 4096 + lane * 16;
        v4u u0, u1, u2, u3;
        Spin sp;
        for (;;) {
          asm volatile("" ::: "memory");
          u0 = ldu(rs, foff); u1 = ldu(rs, foff + 1024); u2 = ldu(rs, foff + 2048); u3 = ldu(rs, foff + 3072);
          if (__all(uok(u0, tag) && uok(u1, tag) && uok(u2, tag) && uok(u3, tag))) break;
          if (sp.expired()) break;
        }
        float gs = swap32_add(u2f(u0.x) + u2f(u1.x), u2f(u0.z) + u2f(u1.z));
        float us = swap32_add(u2f(u2.x) + u2f(u3.x), u2f(u2.z) + u2f(u3.z));
        gs = swap16_add(gs, gs); us = swap16_add(us, us);
        gs += dppf<ROR8>(gs); us += dppf<ROR8>(us);
        hv = gs / (1.f + __expf(-gs)) * us * (1.f - gate);
      }
      STAMP(8);
      const unsigned hh = pkbf(hv, hv);
      const unsigned lo = pkbf(hv - bflo(hh), 0.f);
      if ((lane & 16) == 0) *(LAS unsigned short*)(L + L_AV + ((r0 * 2 + (lane >> 5)) * 4 + (c8 & 3)) * 16 + (4 + (c8 >> 2)) * 2) = (unsigned short)(r0 ? lo : hh);
    }
    lds_set(L, F_AV + w);
    STAMP(9);
    if (blk < 64) lds_wait4<1>(L, F_PUB);
  } else {
    const rsrc_t vrs = mkrs(p.v + rbase, 1 << 20);
    const int frow = 64 * xg + 32 * (w >> 1) + 4 * rho;
    const v2u xf = *(const GAS v2u*)(p.x + (((n16 >> 1) & 1) * 512 + frow) * 2);
    unsigned fE0, fE1, fO0, fO1;
    delta_sel(((n16 >> 1) & 2) | (n16 & 1), 0u, fE0, fE1);
    delta_sel(((n16 >> 1) & 2) | (n16 & 1), 2u, fO0, fO1);
    const int fbytes = hasF ? 512 * 3584 : 0;
    const rsrc_t grs = mkrs((w & 1) ? p.wu : p.wg, fbytes), drs = mkrs(p.wd, fbytes), ors = mkrs(p.wo, 4096 * 1024);
    const int go = frow * 3584 + xj * 128 + c8 * 16;
    const int lo_ = w * 256 + n16 * 16;
    if (w == 0) {
      const int ks = xg >> 1, cb = 2 * xj + ((xg ^ xj) & 1);
      const int row0 = 128 * ks + 16 * rho, abb = (n16 >> 2) & 1;
      const rsrc_t wrs = mkrs(p.wq, 512 * 8192);
      const int wo_ = row0 * 8192 + cb * 128 + c8 * 16;
      const v4u xa = *(const GAS v4u*)(p.x + (abb * 512 + row0) * 2), xb = *(const GAS v4u*)(p.x + (abb * 512 + row0) * 2 + 16);
      SB();
      v4u wr[16];
#pragma unroll
      for (int u = 0; u < 16; ++u) { wr[u] = ldnt(wrs, wo_, u * 8192); SB(); }
      SB();
      __builtin_amdgcn_s_barrier();
      SB();
      __builtin_amdgcn_s_setprio(3);
      tag = load_tag(p.ws);
      f32x4 d0 = {0.f, 0.f, 0.f, 0.f}, d1 = {0.f, 0.f, 0.f, 0.f};
#pragma unroll
      for (int u = 0; u < 16; ++u) {
        const unsigned xs = (u < 8) ? xa[(u >> 1) & 3] : xb[(u >> 1) & 3];
        const unsigned A0 = PERM(xs, (u & 1) ? sO0 : sE0), A1 = PERM(xs, (u & 1) ? sO1 : sE1);
        d0 = mfma16(A0, A1, wr[u].x, wr[u].y, d0);
        d1 = mfma16(A0, A1, wr[u].z, wr[u].w, d1);
      }
      float sv[4];
#pragma unroll
      for (int i = 0; i < 4; ++i) sv[i] = swap32_add(dppf_rows<ROR8, 0xc>(d0[i]), dppf_rows<ROR8, 0xc>(d1[i]));
      STAMP(12);
      const int off = WS_Q + ((((cb >> 3) * 2 + (a4 & 1)) * 4 + ks) * 128 + (cb & 7) * 16 + (a4 >> 1) * 8 + n16) * 16;
      stu(rs, n16 < 8 ? off : OOB, pkbf(sv[0], sv[1]), pkbf(sv[2], sv[3]), tag);
      SB();
      __builtin_amdgcn_s_setprio(0);
      STAMP(13);
    } else if (w == 1) {
      const v4u xa = *(const GAS v4u*)(p.x + lane * 16), xb = *(const GAS v4u*)(p.x + 1024 + lane * 16), gw = *(const GAS v4u*)(p.gw + lane * 16);
      SB();
      __builtin_amdgcn_s_barrier();
      SB();
      tag = load_tag(p.ws);
      float s0 = 0.f, s1 = 0.f;
#pragma unroll
      for (int i = 0; i < 4; ++i) {
        s0 = fmaf(bflo(xa[i]), bflo(gw[i]), s0); s0 = fmaf(bfhi(xa[i]), bfhi(gw[i]), s0);
        s1 = fmaf(bflo(xb[i]), bflo(gw[i]), s1); s1 = fmaf(bfhi(xb[i]), bfhi(gw[i]), s1);
      }
      float sg = swap32_add(s0, s1);
      sg = row_sum(swap16_add(sg, sg));
      const float gt = 1.f / (1.f + __expf(-sg));
      if ((lane & 31) == 0) *(LAS float*)(L + L_GT + (lane >> 5) * 4) = gt;
      lds_set(L, F_G);
    } else {
      if (w == 2) {
        *(LAS unsigned*)(L + L_FLAG + lane * 4) = 0u;
        asm volatile("s_waitcnt lgkmcnt(0)" ::: "memory");
      }
      SB();
      __builtin_amdgcn_s_barrier();
      SB();
      tag = load_tag(p.ws);
    }
    SB();
    lds_wait4<1>(L, F_KISS);
    STAMP(9);
    v4u vr[16], gu[4], wdr[2], wor[4];
#pragma unroll
    for (int u = 0; u < 16; ++u) { vr[u] = ldnt(vrs, lane * 16, wofs + u * 65536); SB(); }
    STAMP(1);
#pragma unroll
    for (int u = 0; u < 4; ++u) { gu[u] = ldnt(grs, go, u * 3584); SB(); }
#pragma unroll
    for (int u = 0; u < 2; ++u) { wdr[u] = ldnt(drs, lo_ + a4 * 8192, (fc >> 3) * 65536 + (fc & 7) * 1024 + u * 32768); SB(); }
#pragma unroll
    for (int u = 0; u < 4; ++u) { wor[u] = ldnt(ors, lo_ + a4 * 1024, oi * 16384 + u * 4096); SB(); }
    STAMP(2);
    lds_wait1<0>(L, F_P + w);
    STAMP(3);
    const v4u pa = *(LAS v4u*)(L + L_P + w * 512 + (n16 >> 2) * 128 + a4 * 32), pb = *(LAS v4u*)(L + L_P + w * 512 + (n16 >> 2) * 128 + a4 * 32 + 16);
    f32x4 o0 = {0.f, 0.f, 0.f, 0.f}, o1 = {0.f, 0.f, 0.f, 0.f};
#pragma unroll
    for (int t = 0; t < 16; ++t) {
      const unsigned ps = (t < 8) ? pa[(t >> 1) & 3] : pb[(t >> 1) & 3];
      const unsigned A0 = PERM(ps, (t & 1) ? sO0 : sE0), A1 = PERM(ps, (t & 1) ? sO1 : sE1);
      o0 = mfma16(A0, A1, vr[t].x, vr[t].y, o0);
      o1 = mfma16(A0, A1, vr[t].z, vr[t].w, o1);
    }
    STAMP(4);
    lds_wait4<0>(L, F_L);
    {
      float M = -INFINITY, mine = 0.f;
#pragma unroll
      for (int ww = 0; ww < 4; ++ww) { const float m = *(LAS float*)(L + L_MX + ww * 16 + a4 * 4); M = fmaxf(M, m); if (ww == w) mine = m; }
      const float f = __builtin_amdgcn_exp2f(mine - M);
      *(LAS f32x4*)(L + L_OM + (w * 512 + a4 * 128 + n16 * 8) * 4) = o0 * f;
      *(LAS f32x4*)(L + L_OM + (w * 512 + a4 * 128 + n16 * 8 + 4) * 4) = o1 * f;
    }
    lds_set(L, F_O + w);
    lds_wait4<0>(L, F_O);
    {
      f32x2 acc = {0.f, 0.f};
#pragma unroll
      for (int ww = 0; ww < 4; ++ww) acc += *(LAS f32x2*)(L + L_OM + (ww * 512 + w * 128 + lane * 2) * 4);
      stu(rs, WS_O + (((xg * 32 + 8 * w + (lane >> 3)) * 32 + xj) * 8 + c8) * 16, f2u(acc.x), f2u(acc.y), tag);
    }
    SB();
    STAMP(5);
    {
      f32x4 d0 = {0.f, 0.f, 0.f, 0.f}, d1 = {0.f, 0.f, 0.f, 0.f};
#pragma unroll
      for (int u = 0; u < 4; ++u) {
        const unsigned xs = xf[u >> 1];
        const unsigned A0 = PERM(xs, (u & 1) ? fO0 : fE0), A1 = PERM(xs, (u & 1) ? fO1 : fE1);
        d0 = mfma16(A0, A1, gu[u].x, gu[u].y, d0);
        d1 = mfma16(A0, A1, gu[u].z, gu[u].w, d1);
      }
      float sv[4];
#pragma unroll
      for (int i = 0; i < 4; ++i) sv[i] = swap32_add(dppf_rows<ROR8, 0xc>(d0[i]), dppf_rows<ROR8, 0xc>(d1[i]));
      const float t1 = dppf<ROR8>(sv[1]), t3 = dppf<ROR8>(sv[3]);
      const float b0 = r0 ? t1 : sv[0], b1 = r0 ? t3 : sv[2];
      const int off = WS_F + (xj * 8 + 2 * a4 + r0) * 4096 + (((w & 1) * 16 + xg * 2 + (w >> 1)) * 8 + c8) * 16;
      stu(rs, hasF ? off : OOB, f2u(b0), f2u(b1), tag);
    }
    STAMP(6);
    lds_wait4<0>(L, F_AV);
    STAMP(7);
    {
      const v4u av = *(LAS v4u*)(L + L_AV + ((n16 >> 2) * 4 + a4) * 16);
      f32x4 d0 = {0.f, 0.f, 0.f, 0.f}, d1 = {0.f, 0.f, 0.f, 0.f};
#pragma unroll
      for (int u = 0; u < 6; ++u) {
        const unsigned xs = av[u >> 1];
        const unsigned A0 = PERM(xs, (u & 1) ? sO0 : sE0), A1 = PERM(xs, (u & 1) ? sO1 : sE1);
        const v4u wv = u < 4 ? wor[u & 3] : wdr[u & 1];
        d0 = mfma16(A0, A1, wv.x, wv.y, d0);
        d1 = mfma16(A0, A1, wv.z, wv.w, d1);
      }
      float sv[4];
#pragma unroll
      for (int i = 0; i < 4; ++i) sv[i] = swap32_add(d0[i], d1[i]);
      const int off = WS_R + (((w * 16 + (lane >> 3)) * 256 + blk) * 8 + c8) * 16;
      stu(rs, off, f2u(sv[0]), f2u(sv[1]), tag);
      stu(rs, off + 8 * 32768, f2u(sv[2]), f2u(sv[3]), tag);
    }
    lds_set(L, F_PUB + w);
    STAMP(8);
  }
  if (blk < 64) {
    const int roff = WS_R + blk * 32768 + tid * 16;
    v4u u0, u1, u2, u3;
    Spin sp;
    for (;;) {
      asm volatile("" ::: "memory");
      u0 = ldu(rs, roff); u1 = ldu(rs, roff + 8192); u2 = ldu(rs, roff + 16384); u3 = ldu(rs, roff + 24576);
      if (__all(uok(u0, tag) && uok(u1, tag) && uok(u2, tag) && uok(u3, tag))) break;
      if (sp.expired()) break;
    }
    STAMP(10);
    float vv = swap32_add((u2f(u0.x) + u2f(u1.x)) + (u2f(u2.x) + u2f(u3.x)), (u2f(u0.z) + u2f(u1.z)) + (u2f(u2.z) + u2f(u3.z)));
    vv = swap16_add(vv, vv); vv += dppf<ROR8>(vv);
    if ((lane & 24) == 0) *(LAS float*)(L + L_RED + wave * 64 + c8 * 8 + (lane >> 5) * 4) = vv;
    __syncthreads();
    if (tid < 8) {
      f32x2 sum = {0.f, 0.f};
#pragma unroll
      for (int ww = 0; ww < 8; ++ww) sum += *(LAS f32x2*)(L + L_RED + ww * 64 + tid * 8);
      const int l = (blk & 7) * 8 + tid, col = 128 * (blk >> 4) + 8 * (l & 15) + 4 * (l >> 5) + 2 * ((blk >> 3) & 1), ob = (l >> 4) & 1;
      *(GAS unsigned*)(p.out + (ob * 512 + col) * 2) = pkbf(sum.x, sum.y);
      if (blk == 0 && tid == 0) __hip_atomic_store((GAS unsigned*)(p.ws + WS_EPOCH), tag, RLX_AGENT);
    }
    STAMP(11);
  }
}

}

extern "C" void kernel_launch(void* const* d_in, const int* in_sizes, int n_in, void* d_out, int out_size, void* d_ws, size_t ws_size,
                              hipStream_t stream) {
  Params p;
  p.x = (const unsigned char*)d_in[0];
  p.k = (const unsigned char*)d_in[1];
  p.v = (const unsigned char*)d_in[2];
  p.wq = (const unsigned char*)d_in[3];
  p.wo = (const unsigned char*)d_in[4];
  p.wg = (const unsigned char*)d_in[6];
  p.wu = (const unsigned char*)d_in[7];
  p.wd = (const unsigned char*)d_in[8];
  p.gw = (const unsigned char*)d_in[9];
  p.out = (unsigned char*)d_out;
  p.ws = (unsigned char*)d_ws;
  hipLaunchKernelGGL(fused_kernel, dim3(256), dim3(512), 0, stream, p);
}
```

```cpp
#include <hip/hip_runtime.h>
#include <hip/hip_bf16.h>
#include <stdint.h>

#define GAS __attribute__((address_space(1)))
#define LAS __attribute__((address_space(3)))
#define RLX_AGENT __ATOMIC_RELAXED, __HIP_MEMORY_SCOPE_AGENT
#define SB() __builtin_amdgcn_sched_barrier(0)
typedef unsigned v4u __attribute__((ext_vector_type(4)));
typedef unsigned v2u __attribute__((ext_vector_type(2)));
typedef float f32x4 __attribute__((ext_vector_type(4)));
typedef float f32x2 __attribute__((ext_vector_type(2)));
typedef __bf16 bf16x8 __attribute__((ext_vector_type(8)));
typedef __bf16 bf16x2 __attribute__((ext_vector_type(2)));
typedef short s16x4 __attribute__((ext_vector_type(4)));

namespace {

constexpr int SK = 32768, WIN = 4096;
constexpr int WS_EPOCH = 0;
constexpr int WS_Q = 4096;
constexpr int WS_ML = WS_Q + 8 * 2 * 4 * 128 * 16;
constexpr int WS_F = WS_ML + 8 * 4 * 32 * 16;
constexpr int WS_O = WS_F + 224 * 4096;
constexpr int WS_R = WS_O + 256 * 4096;
constexpr int WS_END = WS_R + 64 * 32768;
constexpr int WS_DIAG = WS_END;
constexpr int WS_RSRC_BYTES = WS_END;
constexpr int OOB = 0x7fffff00;

constexpr int L_FLAG = 0;
constexpr int L_ZERO = 192;
constexpr int L_QS = 256;
constexpr int L_P = L_QS + 1024;
constexpr int L_MX = L_P + 2048;
constexpr int L_LS = L_MX + 64;
constexpr int L_AV = L_LS + 64;
constexpr int L_GT = L_AV + 256;
constexpr int L_RED = L_GT + 16;
constexpr int L_OM = L_RED + 512;
constexpr int L_BYTES = L_OM + 8192;
constexpr int F_KISS = 0, F_Q = 4, F_P = 8, F_L = 12, F_O = 16, F_AV = 20, F_PUB = 24, F_G = 28;

constexpr unsigned long long TLIMIT = 20000000ull;

struct Params {
  const unsigned char *x, *k, *v, *wq, *wo, *wg, *wu, *wd, *gw;
  unsigned char *out, *ws;
};

typedef __amdgpu_buffer_rsrc_t rsrc_t;
__device__ __forceinline__ rsrc_t mkrs(const unsigned char* base, int bytes) {
  return __builtin_amdgcn_make_buffer_rsrc((void*)base, 0, bytes, 0x00020000);
}
__device__ __forceinline__ v4u ldnt(rsrc_t r, int voff, int soff) { return __builtin_amdgcn_raw_buffer_load_b128(r, voff, soff, 2); }
__device__ __forceinline__ unsigned pkbf(float lo, float hi) {
  bf16x2 r = {(__bf16)lo, (__bf16)hi};
  return __builtin_bit_cast(unsigned, r);
}
__device__ __forceinline__ float bflo(unsigned u) { return __builtin_bit_cast(float, u << 16); }
__device__ __forceinline__ float bfhi(unsigned u) { return __builtin_bit_cast(float, u & 0xffff0000u); }
__device__ __forceinline__ float u2f(unsigned u) { return __builtin_bit_cast(float, u); }
__device__ __forceinline__ unsigned f2u(float f) { return __builtin_bit_cast(unsigned, f); }
__device__ __forceinline__ f32x4 mfma32(v4u a, v4u b, f32x4 c) {
  return __builtin_amdgcn_mfma_f32_16x16x32_bf16(__builtin_bit_cast(bf16x8, a), __builtin_bit_cast(bf16x8, b), c, 0, 0, 0);
}
__device__ __forceinline__ f32x4 mfma16(unsigned a0, unsigned a1, unsigned b0, unsigned b1, f32x4 c) {
  v2u a = {a0, a1}, b = {b0, b1};
  return __builtin_amdgcn_mfma_f32_16x16x16bf16_1k(__builtin_bit_cast(s16x4, a), __builtin_bit_cast(s16x4, b), c, 0, 0, 0);
}
template <int CTRL> __device__ __forceinline__ float dppf(float x) {
  return u2f(__builtin_amdgcn_mov_dpp((int)f2u(x), CTRL, 0xf, 0xf, true));
}
template <int CTRL, int ROWMASK> __device__ __forceinline__ float dppf_rows(float x) {
  return u2f(__builtin_amdgcn_update_dpp((int)f2u(x), (int)f2u(x), CTRL, ROWMASK, 0xf, false));
}
constexpr int ROR8 = 0x128, ROR4 = 0x124, ROR2 = 0x122, ROR1 = 0x121;
__device__ __forceinline__ float swap32_add(float a, float b) {
  auto r = __builtin_amdgcn_permlane32_swap(f2u(a), f2u(b), false, false);
  return u2f(r[0]) + u2f(r[1]);
}
__device__ __forceinline__ float swap32_max(float a, float b) {
  auto r = __builtin_amdgcn_permlane32_swap(f2u(a), f2u(b), false, false);
  return fmaxf(u2f(r[0]), u2f(r[1]));
}
__device__ __forceinline__ float swap16_add(float a, float b) {
  auto r = __builtin_amdgcn_permlane16_swap(f2u(a), f2u(b), false, false);
  return u2f(r[0]) + u2f(r[1]);
}
__device__ __forceinline__ float swap16_max(float a, float b) {
  auto r = __builtin_amdgcn_permlane16_swap(f2u(a), f2u(b), false, false);
  return fmaxf(u2f(r[0]), u2f(r[1]));
}
__device__ __forceinline__ float xrow_max(float x) { x = swap16_max(x, x); return swap32_max(x, x); }
__device__ __forceinline__ float xrow_sum(float x) { x = swap16_add(x, x); return swap32_add(x, x); }
__device__ __forceinline__ float row_max(float x) {
  x = fmaxf(x, dppf<ROR8>(x)); x = fmaxf(x, dppf<ROR4>(x)); x = fmaxf(x, dppf<ROR2>(x)); return fmaxf(x, dppf<ROR1>(x));
}
__device__ __forceinline__ float row_sum(float x) {
  x += dppf<ROR8>(x); x += dppf<ROR4>(x); x += dppf<ROR2>(x); return x + dppf<ROR1>(x);
}
__device__ __forceinline__ float rdlane(float x, int l) { return u2f(__builtin_amdgcn_readlane(f2u(x), l)); }
#define LD_AUX 16
#define ST_AUX 16
__device__ __forceinline__ v4u ldu(rsrc_t rs, int off) { return __builtin_amdgcn_raw_buffer_load_b128(rs, off, 0, LD_AUX); }
__device__ __forceinline__ void stu(rsrc_t rs, int off, unsigned v0, unsigned v1, unsigned tag) {
  v4u u = {v0, tag, v1, tag};
  __builtin_amdgcn_raw_buffer_store_b128(u, rs, off, 0, ST_AUX);
}
__device__ __forceinline__ bool uok(v4u u, unsigned tag) { return u.y == tag && u.w == tag; }

__device__ __forceinline__ unsigned load_tag(const unsigned char* ws) {
  unsigned e;
  asm volatile("s_load_dword %0, %1, 0x0\n\ts_waitcnt lgkmcnt(0)" : "=s"(e) : "s"(ws) : "memory");
  SB();
  return e + 1u;
}

struct Spin {
  unsigned n = 0; unsigned long long t0 = 0;
  __device__ __forceinline__ bool expired() {
    if ((++n & 255u) != 0u) return false;
    const unsigned long long t = __builtin_amdgcn_s_memrealtime();
    if (t0 == 0) { t0 = t; return false; }
    return t - t0 > TLIMIT;
  }
};
template <int SLEEP> __device__ __forceinline__ void lds_wait4(LAS unsigned char* L, int grp) {
  Spin sp;
  for (;;) {
    const v4u f = *(volatile LAS v4u*)(L + L_FLAG + grp * 4);
    if (__builtin_amdgcn_readfirstlane(f.x & f.y & f.z & f.w) != 0u) break;
    if (sp.expired()) break;
    if (SLEEP) __builtin_amdgcn_s_sleep(SLEEP);
  }
  asm volatile("" ::: "memory");
}
template <int SLEEP> __device__ __forceinline__ void lds_wait1(LAS unsigned char* L, int word) {
  Spin sp;
  for (;;) {
    const unsigned f = *(volatile LAS unsigned*)(L + L_FLAG + word * 4);
    if (__builtin_amdgcn_readfirstlane(f) != 0u) break;
    if (sp.expired()) break;
    if (SLEEP) __builtin_amdgcn_s_sleep(SLEEP);
  }
  asm volatile("" ::: "memory");
}
__device__ __forceinline__ void lds_set(LAS unsigned char* L, int word) {
  asm volatile("" ::: "memory");
  *(volatile LAS unsigned*)(L + L_FLAG + word * 4) = 1u;
}
__device__ __forceinline__ void delta_sel(int jj, unsigned sb, unsigned& s0, unsigned& s1) {
  const unsigned pr = sb | ((sb + 1u) << 8);
  const unsigned lo = 0x0c0c0000u | pr, hi = (pr << 16) | 0x0c0cu, z = 0x0c0c0c0cu;
  s0 = jj == 0 ? lo : (jj == 1 ? hi : z);
  s1 = jj == 2 ? lo : (jj == 3 ? hi : z);
}
#define PERM(x, s) __builtin_amdgcn_perm(0u, (x), (s))

#define STAMP(k) do { if ((k) == 0 || ((k) == 0 && (wave >> 2) == 0)) { asm volatile("s_memrealtime %0\n\ts_waitcnt lgkmcnt(0)" : "=s"(probe_t) :: "memory"); } } while (0)

__global__ void __launch_bounds__(512, 2) fused_kernel(Params p) {
  __shared__ __attribute__((aligned(16))) unsigned char lds_raw[L_BYTES];
  LAS unsigned char* L = (LAS unsigned char*)lds_raw;
  const int tid = threadIdx.x, lane = tid & 63, wave = __builtin_amdgcn_readfirstlane(tid >> 6);
  const int blk = blockIdx.x, xg = blk & 7, xj = blk >> 3;
  const int n16 = lane & 15, a4 = lane >> 4, w = wave & 3;
  const int c8 = lane & 7, r0 = (lane >> 3) & 1, rho = 2 * a4 + r0;
  const rsrc_t rs = mkrs(p.ws, WS_RSRC_BYTES);
  const int ab = xj >> 4, as = xj & 15;
  const size_t rbase = ((size_t)(ab * 8 + xg) * SK + (SK - WIN)) * 256;
  const int wofs = (4 * as + w) * 1024;
  const bool hasF = xj < 28;
  const int fc = xg * 28 + xj, oi = xg * 32 + xj;
  asm volatile("" :: "s"(p.x), "s"(p.k), "s"(p.v), "s"(p.wq), "s"(p.wo), "s"(p.wg), "s"(p.wu), "s"(p.wd), "s"(p.gw), "s"(p.out), "s"(p.ws));
  SB();
  unsigned long long probe_t;
  STAMP(0);
  unsigned sE0, sE1, sO0, sO1;
  delta_sel(n16 & 3, 0u, sE0, sE1);
  delta_sel(n16 & 3, 2u, sO0, sO1);
  unsigned tag;

  if (wave < 4) {
    const rsrc_t krs = mkrs(p.k + rbase, 1 << 20);
    const int ko0 = c8 * 65536 + a4 * 16 + r0 * 64, ko1 = c8 * 65536 + a4 * 16 + (r0 ^ 1) * 64 + 524288;
    const int qoff = WS_Q + (((xg * 2 + ab) * 4 + 2 * (lane >> 5)) * 128 + w * 32 + (lane & 31)) * 16;
    v4u kr[16];
    SB();
    __builtin_amdgcn_s_barrier();
    SB();
#pragma unroll
    for (int u = 0; u < 16; ++u) {
      kr[u] = ldnt(krs, ((u >> 3) ? ko1 : ko0) + ((u >> 1) & 3) * 256 + (u & 1) * 128, wofs);
      SB();
    }
    lds_set(L, F_KISS + w);
    STAMP(1);
    SB();
    tag = load_tag(p.ws);
    SB();
    STAMP(2);
    v4u qp[4][2];
#pragma unroll
    for (int r = 0; r < 4; ++r) {
      asm volatile("" :: "v"(kr[4 * r + 3]));
      SB();
      qp[r][0] = ldu(rs, qoff); qp[r][1] = ldu(rs, qoff + 2048);
      SB();
    }
    STAMP(3);
    v4u q0 = qp[0][0], q1 = qp[0][1];
    bool got = __all(uok(q0, tag) && uok(q1, tag));
#pragma unroll
    for (int r = 1; r < 4; ++r) {
      if (!got) { q0 = qp[r][0]; q1 = qp[r][1]; got = __all(uok(q0, tag) && uok(q1, tag)); }
    }
    if (!got) {
      Spin sp;
      for (;;) {
        asm volatile("" ::: "memory");
        q0 = ldu(rs, qoff); q1 = ldu(rs, qoff + 2048);
        if (__all(uok(q0, tag) && uok(q1, tag))) break;
        if (sp.expired()) break;
      }
    }
    STAMP(4);
    {
      constexpr float QS = 0.088388347648318440f * 1.4426950408889634f;
      const float c0 = bflo(q0.x) + bflo(q1.x), c1 = bfhi(q0.x) + bfhi(q1.x), c2 = bflo(q0.z) + bflo(q1.z), c3 = bfhi(q0.z) + bfhi(q1.z);
      const float e0 = swap32_add(c0, c2) * QS, e1 = swap32_add(c1, c3) * QS;
      const int pos = lane & 31;
      const int dim = (pos >> 4) * 64 + (pos & 7) * 8 + ((pos >> 3) & 1) * 4 + (lane >> 5) * 2;
      *(LAS unsigned*)(L + L_QS + (w * 128 + dim) * 2) = pkbf(e0, e1);
    }
    lds_set(L, F_Q + w);
    lds_wait4<0>(L, F_Q);
    const int qa = L_QS + ((n16 & 3) * 128 + ((n16 >> 2) & 1) * 32 + a4 * 8) * 2;
    const int qaa = n16 < 8 ? qa : L_ZERO - 128, qab = n16 < 8 ? L_ZERO - 128 : qa;
    const v4u qa0 = *(LAS v4u*)(L + (n16 < 8 ? qa : L_ZERO)), qa1 = *(LAS v4u*)(L + qaa + 128);
    const v4u qb0 = *(LAS v4u*)(L + (n16 < 8 ? L_ZERO : qa)), qb1 = *(LAS v4u*)(L + qab + 128);
    f32x4 acc[2][2];
#pragma unroll
    for (int e = 0; e < 2; ++e)
#pragma unroll
      for (int P = 0; P < 2; ++P) {
        f32x4 a = {0.f, 0.f, 0.f, 0.f};
        a = mfma32(qa0, kr[e * 8 + P * 2], a); a = mfma32(qa1, kr[e * 8 + P * 2 + 1], a);
        a = mfma32(qb0, kr[e * 8 + P * 2 + 4], a); a = mfma32(qb1, kr[e * 8 + P * 2 + 5], a);
        acc[e][P] = a;
      }
    const bool sel0 = ((a4 & 1) == (n16 >> 3));
    float S[4];
#pragma unroll
    for (int i = 0; i < 4; ++i) {
      const float y0 = dppf_rows<ROR8, 0xa>(sel0 ? acc[0][0][i] : acc[1][0][i]);
      const float y1 = dppf_rows<ROR8, 0xa>(sel0 ? acc[0][1][i] : acc[1][1][i]);
      S[i] = swap16_add(y0, y1);
    }
    const float mrow = row_max(swap16_max(swap32_max(S[0], S[1]), swap32_max(S[2], S[3])));
    const float m0 = rdlane(mrow, 0), m2 = rdlane(mrow, 16), m1 = rdlane(mrow, 32), m3 = rdlane(mrow, 48);
    const unsigned P01 = pkbf(__builtin_amdgcn_exp2f(S[0] - m0), __builtin_amdgcn_exp2f(S[1] - m1));
    const unsigned P23 = pkbf(__builtin_amdgcn_exp2f(S[2] - m2), __builtin_amdgcn_exp2f(S[3] - m3));
    {
      LAS unsigned short* pp = (LAS unsigned short*)(L + L_P + w * 512 + lane * 2);
      pp[0] = (unsigned short)P01; pp[64] = (unsigned short)(P01 >> 16); pp[128] = (unsigned short)P23; pp[192] = (unsigned short)(P23 >> 16);
    }
    lds_set(L, F_P + w);
    STAMP(5);
    {
      const float lrow = row_sum(swap16_add(swap32_add(bflo(P01), bfhi(P01)), swap32_add(bflo(P23), bfhi(P23))));
      const int hrow = ((a4 & 1) << 1) | (a4 >> 1);
      if (n16 == 0) { *(LAS float*)(L + L_MX + w * 16 + hrow * 4) = mrow; *(LAS float*)(L + L_LS + w * 16 + hrow * 4) = lrow; }
    }
    lds_set(L, F_L + w);
    lds_wait4<0>(L, F_L);
    {
      float mw[4], lw[4], M = -INFINITY, lsum = 0.f;
#pragma unroll
      for (int ww = 0; ww < 4; ++ww) { mw[ww] = *(LAS float*)(L + L_MX + ww * 16 + w * 4); lw[ww] = *(LAS float*)(L + L_LS + ww * 16 + w * 4); M = fmaxf(M, mw[ww]); }
#pragma unroll
      for (int ww = 0; ww < 4; ++ww) lsum = fmaf(__builtin_amdgcn_exp2f(mw[ww] - M), lw[ww], lsum);
      stu(rs, lane == 0 ? WS_ML + ((xg * 4 + w) * 32 + xj) * 16 : OOB, f2u(M), f2u(lsum), tag);
    }
#pragma unroll
    for (int r = 0; r < 4; ++r) asm volatile("" :: "v"(qp[r][0]), "v"(qp[r][1]));
    STAMP(6);
    lds_wait1<1>(L, F_G);
    if ((w & 1) == 0) {
      const int bb = w >> 1;
      const float gate = *(LAS float*)(L + L_GT + bb * 4);
      const int mloff = WS_ML + ((xg * 4 + (xj >> 3)) * 32 + bb * 16 + (lane >> 3)) * 16;
      v4u ml0, ml1;
      {
        Spin sp;
        for (;;) {
          asm volatile("" ::: "memory");
          ml0 = ldu(rs, mloff); ml1 = ldu(rs, mloff + 128);
          if (__all(uok(ml0, tag) && uok(ml1, tag))) break;
          if (sp.expired()) break;
        }
      }
      float mm = fmaxf(u2f(ml0.x), u2f(ml1.x));
      mm = fmaxf(mm, dppf<ROR8>(mm)); mm = xrow_max(mm);
      const float e0 = __builtin_amdgcn_exp2f(u2f(ml0.x) - mm), e1 = __builtin_amdgcn_exp2f(u2f(ml1.x) - mm);
      float ll = e0 * u2f(ml0.z) + e1 * u2f(ml1.z);
      ll += dppf<ROR8>(ll); ll = xrow_sum(ll);
      const float sc = gate / ll, w0 = e0 * sc, w1 = e1 * sc;
      STAMP(7);
      const int ooff = WS_O + ((oi * 32 + bb * 16) * 8 + lane) * 16;
      v4u u0, u1;
      {
        Spin sp;
        for (;;) {
          asm volatile("" ::: "memory");
          u0 = ldu(rs, ooff); u1 = ldu(rs, ooff + 1024);
          if (__all(uok(u0, tag) && uok(u1, tag))) break;
          if (sp.expired()) break;
        }
      }
      STAMP(8);
      const float v0 = w0 * u2f(u0.x) + w1 * u2f(u1.x), v1 = w0 * u2f(u0.z) + w1 * u2f(u1.z);
      float vv = swap32_add(v0, v1);
      vv = swap16_add(vv, vv); vv += dppf<ROR8>(vv);
      const unsigned hh = pkbf(vv, vv);
      const unsigned lo = pkbf(vv - bflo(hh), 0.f);
      const int r = 2 * c8 + (lane >> 5);
      if ((lane & 16) == 0) *(LAS unsigned short*)(L + L_AV + ((r0 * 2 + bb) * 4 + (r & 3)) * 16 + (r >> 2) * 2) = (unsigned short)(r0 ? lo : hh);
    } else if (w == 1) {
      float hv = 0.f;
      const float gate = *(LAS float*)(L + L_GT + (lane >> 5) * 4);
      if (hasF) {
        const int foff = WS_F + fc * 4096 + lane * 16;
        v4u u0, u1, u2, u3;
        Spin sp;
        for (;;) {
          asm volatile("" ::: "memory");
          u0 = ldu(rs, foff); u1 = ldu(rs, foff + 1024); u2 = ldu(rs, foff + 2048); u3 = ldu(rs, foff + 3072);
          if (__all(uok(u0, tag) && uok(u1, tag) && uok(u2, tag) && uok(u3, tag))) break;
          if (sp.expired()) break;
        }
        float gs = swap32_add(u2f(u0.x) + u2f(u1.x), u2f(u0.z) + u2f(u1.z));
        float us = swap32_add(u2f(u2.x) + u2f(u3.x), u2f(u2.z) + u2f(u3.z));
        gs = swap16_add(gs, gs); us = swap16_add(us, us);
        gs += dppf<ROR8>(gs); us += dppf<ROR8>(us);
        hv = gs / (1.f + __expf(-gs)) * us * (1.f - gate);
      }
      STAMP(8);
      const unsigned hh = pkbf(hv, hv);
      const unsigned lo = pkbf(hv - bflo(hh), 0.f);
      if ((lane & 16) == 0) *(LAS unsigned short*)(L + L_AV + ((r0 * 2 + (lane >> 5)) * 4 + (c8 & 3)) * 16 + (4 + (c8 >> 2)) * 2) = (unsigned short)(r0 ? lo : hh);
    }
    lds_set(L, F_AV + w);
    STAMP(9);
    if (blk < 64) lds_wait4<1>(L, F_PUB);
  } else {
    const rsrc_t vrs = mkrs(p.v + rbase, 1 << 20);
    const int frow = 64 * xg + 32 * (w >> 1) + 4 * rho;
    const v2u xf = *(const GAS v2u*)(p.x + (((n16 >> 1) & 1) * 512 + frow) * 2);
    unsigned fE0, fE1, fO0, fO1;
    delta_sel(((n16 >> 1) & 2) | (n16 & 1), 0u, fE0, fE1);
    delta_sel(((n16 >> 1) & 2) | (n16 & 1), 2u, fO0, fO1);
    const int fbytes = hasF ? 512 * 3584 : 0;
    const rsrc_t grs = mkrs((w & 1) ? p.wu : p.wg, fbytes), drs = mkrs(p.wd, fbytes), ors = mkrs(p.wo, 4096 * 1024);
    const int go = frow * 3584 + xj * 128 + c8 * 16;
    const int lo_ = w * 256 + n16 * 16;
    if (w == 0) {
      const int ks = xg >> 1, cb = 2 * xj + ((xg ^ xj) & 1);
      const int row0 = 128 * ks + 16 * rho, abb = (n16 >> 2) & 1;
      const rsrc_t wrs = mkrs(p.wq, 512 * 8192);
      const int wo_ = row0 * 8192 + cb * 128 + c8 * 16;
      const v4u xa = *(const GAS v4u*)(p.x + (abb * 512 + row0) * 2), xb = *(const GAS v4u*)(p.x + (abb * 512 + row0) * 2 + 16);
      SB();
      v4u wr[16];
#pragma unroll
      for (int u = 0; u < 16; ++u) { wr[u] = ldnt(wrs, wo_, u * 8192); SB(); }
      SB();
      __builtin_amdgcn_s_barrier();
      SB();
      __builtin_amdgcn_s_setprio(3);
      tag = load_tag(p.ws);
      f32x4 d0 = {0.f, 0.f, 0.f, 0.f}, d1 = {0.f, 0.f, 0.f, 0.f};
#pragma unroll
      for (int u = 0; u < 16; ++u) {
        const unsigned xs = (u < 8) ? xa[(u >> 1) & 3] : xb[(u >> 1) & 3];
        const unsigned A0 = PERM(xs, (u & 1) ? sO0 : sE0), A1 = PERM(xs, (u & 1) ? sO1 : sE1);
        d0 = mfma16(A0, A1, wr[u].x, wr[u].y, d0);
        d1 = mfma16(A0, A1, wr[u].z, wr[u].w, d1);
      }
      float sv[4];
#pragma unroll
      for (int i = 0; i < 4; ++i) sv[i] = swap32_add(dppf_rows<ROR8, 0xc>(d0[i]), dppf_rows<ROR8, 0xc>(d1[i]));
      STAMP(12);
      const int off = WS_Q + ((((cb >> 3) * 2 + (a4 & 1)) * 4 + ks) * 128 + (cb & 7) * 16 + (a4 >> 1) * 8 + n16) * 16;
      stu(rs, n16 < 8 ? off : OOB, pkbf(sv[0], sv[1]), pkbf(sv[2], sv[3]), tag);
      SB();
      __builtin_amdgcn_s_setprio(0);
      STAMP(13);
    } else if (w == 1) {
      const v4u xa = *(const GAS v4u*)(p.x + lane * 16), xb = *(const GAS v4u*)(p.x + 1024 + lane * 16), gw = *(const GAS v4u*)(p.gw + lane * 16);
      SB();
      __builtin_amdgcn_s_barrier();
      SB();
      tag = load_tag(p.ws);
      float s0 = 0.f, s1 = 0.f;
#pragma unroll
      for (int i = 0; i < 4; ++i) {
        s0 = fmaf(bflo(xa[i]), bflo(gw[i]), s0); s0 = fmaf(bfhi(xa[i]), bfhi(gw[i]), s0);
        s1 = fmaf(bflo(xb[i]), bflo(gw[i]), s1); s1 = fmaf(bfhi(xb[i]), bfhi(gw[i]), s1);
      }
      float sg = swap32_add(s0, s1);
      sg = row_sum(swap16_add(sg, sg));
      const float gt = 1.f / (1.f + __expf(-sg));
      if ((lane & 31) == 0) *(LAS float*)(L + L_GT + (lane >> 5) * 4) = gt;
      lds_set(L, F_G);
    } else {
      if (w == 2) {
        *(LAS unsigned*)(L + L_FLAG + lane * 4) = 0u;
        asm volatile("s_waitcnt lgkmcnt(0)" ::: "memory");
      }
      SB();
      __builtin_amdgcn_s_barrier();
      SB();
      tag = load_tag(p.ws);
    }
    SB();
    lds_wait4<1>(L, F_KISS);
    STAMP(9);
    v4u vr[16], gu[4], wdr[2], wor[4];
#pragma unroll
    for (int u = 0; u < 16; ++u) { vr[u] = ldnt(vrs, lane * 16, wofs + u * 65536); SB(); }
    STAMP(1);
#pragma unroll
    for (int u = 0; u < 4; ++u) { gu[u] = ldnt(grs, go, u * 3584); SB(); }
#pragma unroll
    for (int u = 0; u < 2; ++u) { wdr[u] = ldnt(drs, lo_ + a4 * 8192, (fc >> 3) * 65536 + (fc & 7) * 1024 + u * 32768); SB(); }
#pragma unroll
    for (int u = 0; u < 4; ++u) { wor[u] = ldnt(ors, lo_ + a4 * 1024, oi * 16384 + u * 4096); SB(); }
    STAMP(2);
    lds_wait1<0>(L, F_P + w);
    STAMP(3);
    const v4u pa = *(LAS v4u*)(L + L_P + w * 512 + (n16 >> 2) * 128 + a4 * 32), pb = *(LAS v4u*)(L + L_P + w * 512 + (n16 >> 2) * 128 + a4 * 32 + 16);
    f32x4 o0 = {0.f, 0.f, 0.f, 0.f}, o1 = {0.f, 0.f, 0.f, 0.f};
#pragma unroll
    for (int t = 0; t < 16; ++t) {
      const unsigned ps = (t < 8) ? pa[(t >> 1) & 3] : pb[(t >> 1) & 3];
      const unsigned A0 = PERM(ps, (t & 1) ? sO0 : sE0), A1 = PERM(ps, (t & 1) ? sO1 : sE1);
      o0 = mfma16(A0, A1, vr[t].x, vr[t].y, o0);
      o1 = mfma16(A0, A1, vr[t].z, vr[t].w, o1);
    }
    STAMP(4);
    lds_wait4<0>(L, F_L);
    {
      float M = -INFINITY, mine = 0.f;
#pragma unroll
      for (int ww = 0; ww < 4; ++ww) { const float m = *(LAS float*)(L + L_MX + ww * 16 + a4 * 4); M = fmaxf(M, m); if (ww == w) mine = m; }
      const float f = __builtin_amdgcn_exp2f(mine - M);
      *(LAS f32x4*)(L + L_OM + (w * 512 + a4 * 128 + n16 * 8) * 4) = o0 * f;
      *(LAS f32x4*)(L + L_OM + (w * 512 + a4 * 128 + n16 * 8 + 4) * 4) = o1 * f;
    }
    lds_set(L, F_O + w);
    lds_wait4<0>(L, F_O);
    {
      f32x2 acc = {0.f, 0.f};
#pragma unroll
      for (int ww = 0; ww < 4; ++ww) acc += *(LAS f32x2*)(L + L_OM + (ww * 512 + w * 128 + lane * 2) * 4);
      stu(rs, WS_O + (((xg * 32 + 8 * w + (lane >> 3)) * 32 + xj) * 8 + c8) * 16, f2u(acc.x), f2u(acc.y), tag);
    }
    SB();
    STAMP(5);
    {
      f32x4 d0 = {0.f, 0.f, 0.f, 0.f}, d1 = {0.f, 0.f, 0.f, 0.f};
#pragma unroll
      for (int u = 0; u < 4; ++u) {
        const unsigned xs = xf[u >> 1];
        const unsigned A0 = PERM(xs, (u & 1) ? fO0 : fE0), A1 = PERM(xs, (u & 1) ? fO1 : fE1);
        d0 = mfma16(A0, A1, gu[u].x, gu[u].y, d0);
        d1 = mfma16(A0, A1, gu[u].z, gu[u].w, d1);
      }
      float sv[4];
#pragma unroll
      for (int i = 0; i < 4; ++i) sv[i] = swap32_add(dppf_rows<ROR8, 0xc>(d0[i]), dppf_rows<ROR8, 0xc>(d1[i]));
      const float t1 = dppf<ROR8>(sv[1]), t3 = dppf<ROR8>(sv[3]);
      const float b0 = r0 ? t1 : sv[0], b1 = r0 ? t3 : sv[2];
      const int off = WS_F + (xj * 8 + 2 * a4 + r0) * 4096 + (((w & 1) * 16 + xg * 2 + (w >> 1)) * 8 + c8) * 16;
      stu(rs, hasF ? off : OOB, f2u(b0), f2u(b1), tag);
    }
    STAMP(6);
    lds_wait4<0>(L, F_AV);
    STAMP(7);
    {
      const v4u av = *(LAS v4u*)(L + L_AV + ((n16 >> 2) * 4 + a4) * 16);
      f32x4 d0 = {0.f, 0.f, 0.f, 0.f}, d1 = {0.f, 0.f, 0.f, 0.f};
#pragma unroll
      for (int u = 0; u < 6; ++u) {
        const unsigned xs = av[u >> 1];
        const unsigned A0 = PERM(xs, (u & 1) ? sO0 : sE0), A1 = PERM(xs, (u & 1) ? sO1 : sE1);
        const v4u wv = u < 4 ? wor[u & 3] : wdr[u & 1];
        d0 = mfma16(A0, A1, wv.x, wv.y, d0);
        d1 = mfma16(A0, A1, wv.z, wv.w, d1);
      }
      float sv[4];
#pragma unroll
      for (int i = 0; i < 4; ++i) sv[i] = swap32_add(d0[i], d1[i]);
      const int off = WS_R + (((w * 16 + (lane >> 3)) * 256 + blk) * 8 + c8) * 16;
      stu(rs, off, f2u(sv[0]), f2u(sv[1]), tag);
      stu(rs, off + 8 * 32768, f2u(sv[2]), f2u(sv[3]), tag);
    }
    lds_set(L, F_PUB + w);
    STAMP(8);
  }
  if (blk < 64) {
    const int roff = WS_R + blk * 32768 + tid * 16;
    v4u u0, u1, u2, u3;
    Spin sp;
    for (;;) {
      asm volatile("" ::: "memory");
      u0 = ldu(rs, roff); u1 = ldu(rs, roff + 8192); u2 = ldu(rs, roff + 16384); u3 = ldu(rs, roff + 24576);
      if (__all(uok(u0, tag) && uok(u1, tag) && uok(u2, tag) && uok(u3, tag))) break;
      if (sp.expired()) break;
    }
    STAMP(10);
    float vv = swap32_add((u2f(u0.x) + u2f(u1.x)) + (u2f(u2.x) + u2f(u3.x)), (u2f(u0.z) + u2f(u1.z)) + (u2f(u2.z) + u2f(u3.z)));
    vv = swap16_add(vv, vv); vv += dppf<ROR8>(vv);
    if ((lane & 24) == 0) *(LAS float*)(L + L_RED + wave * 64 + c8 * 8 + (lane >> 5) * 4) = vv;
    __syncthreads();
    if (tid < 8) {
      f32x2 sum = {0.f, 0.f};
#pragma unroll
      for (int ww = 0; ww < 8; ++ww) sum += *(LAS f32x2*)(L + L_RED + ww * 64 + tid * 8);
      const int l = (blk & 7) * 8 + tid, col = 128 * (blk >> 4) + 8 * (l & 15) + 4 * (l >> 5) + 2 * ((blk >> 3) & 1), ob = (l >> 4) & 1;
      *(GAS unsigned*)(p.out + (ob * 512 + col) * 2) = pkbf(sum.x, sum.y);
      if (blk == 0 && tid == 0) __hip_atomic_store((GAS unsigned*)(p.ws + WS_EPOCH), tag, RLX_AGENT);
    }
    STAMP(11);
  }
  if ((blk & 7) == 7) while (__builtin_amdgcn_s_memrealtime() - probe_t < 4000ull) __builtin_amdgcn_s_sleep(2);
}

}

extern "C" void kernel_launch(void* const* d_in, const int* in_sizes, int n_in, void* d_out, int out_size, void* d_ws, size_t ws_size,
                              hipStream_t stream) {
  Params p;
  p.x = (const unsigned char*)d_in[0];
  p.k = (const unsigned char*)d_in[1];
  p.v = (const unsigned char*)d_in[2];
  p.wq = (const unsigned char*)d_in[3];
  p.wo = (const unsigned char*)d_in[4];
  p.wg = (const unsigned char*)d_in[6];
  p.wu = (const unsigned char*)d_in[7];
  p.wd = (const unsigned char*)d_in[8];
  p.gw = (const unsigned char*)d_in[9];
  p.out = (unsigned char*)d_out;
  p.ws = (unsigned char*)d_ws;
  hipLaunchKernelGGL(fused_kernel, dim3(256), dim3(512), 0, stream, p);
}
```

```cpp
#include <hip/hip_runtime.h>
#include <hip/hip_bf16.h>
#include <stdint.h>

#define GAS __attribute__((address_space(1)))
#define LAS __attribute__((address_space(3)))
#define RLX_AGENT __ATOMIC_RELAXED, __HIP_MEMORY_SCOPE_AGENT
#define SB() __builtin_amdgcn_sched_barrier(0)
typedef unsigned v4u __attribute__((ext_vector_type(4)));
typedef unsigned v2u __attribute__((ext_vector_type(2)));
typedef float f32x4 __attribute__((ext_vector_type(4)));
typedef float f32x2 __attribute__((ext_vector_type(2)));
typedef __bf16 bf16x8 __attribute__((ext_vector_type(8)));
typedef __bf16 bf16x2 __attribute__((ext_vector_type(2)));
typedef short s16x4 __attribute__((ext_vector_type(4)));

namespace {

constexpr int NWAVES = 12;
constexpr int WS_EPOCH = 0;
constexpr int WS_Q = 4096;
constexpr int WS_ML = WS_Q + 8 * 2 * 4 * 128 * 16;
constexpr int WS_F = WS_ML + 8 * 4 * 32 * 16;
constexpr int WS_O = WS_F + 112 * 4096;
constexpr int WS_R = WS_O + 128 * 4096;
constexpr int WS_END = WS_R + 64 * 8192;
constexpr int WS_DIAG = WS_END;
constexpr int WS_RSRC_BYTES = WS_END;
constexpr int OOB = 0x7fffff00;

constexpr int L_FLAG = 0;
constexpr int L_ZERO = 192;
constexpr int L_QS = 256;
constexpr int L_P = L_QS + 1024;
constexpr int L_MX = L_P + 2048;
constexpr int L_LS = L_MX + 64;
constexpr int L_AV = L_LS + 64;
constexpr int L_GT = L_AV + 256;
constexpr int L_RED = L_GT + 16;
constexpr int L_OM = L_RED + 1024;
constexpr int L_BYTES = L_OM + 8192;
constexpr int F_KISS = 0, F_Q = 4, F_P = 8, F_L = 12, F_O = 16, F_AV = 20, F_PUB = 24, F_G = 28, F_QP = 29, F_FP = 32, F_VISS = 36, F_GU = 40, F_FT = 44;

constexpr unsigned long long TLIMIT = 20000000ull;

struct Params {
  const unsigned char *x, *wq, *k, *v, *wo, *wg, *wu, *wd, *gw;
  unsigned char *out, *ws;
};

typedef __amdgpu_buffer_rsrc_t rsrc_t;
__device__ __forceinline__ rsrc_t mkrs(const unsigned char* base, int bytes) {
  return __builtin_amdgcn_make_buffer_rsrc((void*)base, 0, bytes, 0x00020000);
}
__device__ __forceinline__ v4u ldnt(rsrc_t r, int voff, int soff) { return __builtin_amdgcn_raw_buffer_load_b128(r, voff, soff, 2); }
__device__ __forceinline__ unsigned pkbf(float lo, float hi) {
  bf16x2 r = {(__bf16)lo, (__bf16)hi};
  return __builtin_bit_cast(unsigned, r);
}
__device__ __forceinline__ float bflo(unsigned u) { return __builtin_bit_cast(float, u << 16); }
__device__ __forceinline__ float bfhi(unsigned u) { return __builtin_bit_cast(float, u & 0xffff0000u); }
__device__ __forceinline__ float u2f(unsigned u) { return __builtin_bit_cast(float, u); }
__device__ __forceinline__ unsigned f2u(float f) { return __builtin_bit_cast(unsigned, f); }
__device__ __forceinline__ f32x4 mfma32(v4u a, v4u b, f32x4 c) {
  return __builtin_amdgcn_mfma_f32_16x16x32_bf16(__builtin_bit_cast(bf16x8, a), __builtin_bit_cast(bf16x8, b), c, 0, 0, 0);
}
__device__ __forceinline__ f32x4 mfma16(unsigned a0, unsigned a1, unsigned b0, unsigned b1, f32x4 c) {
  v2u a = {a0, a1}, b = {b0, b1};
  return __builtin_amdgcn_mfma_f32_16x16x16bf16_1k(__builtin_bit_cast(s16x4, a), __builtin_bit_cast(s16x4, b), c, 0, 0, 0);
}
template <int CTRL> __device__ __forceinline__ float dppf(float x) {
  return u2f(__builtin_amdgcn_mov_dpp((int)f2u(x), CTRL, 0xf, 0xf, true));
}
template <int CTRL, int ROWMASK> __device__ __forceinline__ float dppf_rows(float x) {
  return u2f(__builtin_amdgcn_update_dpp((int)f2u(x), (int)f2u(x), CTRL, ROWMASK, 0xf, false));
}
constexpr int ROR8 = 0x128, ROR4 = 0x124, ROR2 = 0x122, ROR1 = 0x121;
__device__ __forceinline__ float vmax(float a, float b) { return __builtin_amdgcn_fmed3f(a, b, 3.4028234e38f); }
__device__ __forceinline__ float swap32_add(float a, float b) {
  auto r = __builtin_amdgcn_permlane32_swap(f2u(a), f2u(b), false, false);
  return u2f(r[0]) + u2f(r[1]);
}
__device__ __forceinline__ float swap32_max(float a, float b) {
  auto r = __builtin_amdgcn_permlane32_swap(f2u(a), f2u(b), false, false);
  return vmax(u2f(r[0]), u2f(r[1]));
}
__device__ __forceinline__ float swap16_add(float a, float b) {
  auto r = __builtin_amdgcn_permlane16_swap(f2u(a), f2u(b), false, false);
  return u2f(r[0]) + u2f(r[1]);
}
__device__ __forceinline__ float swap16_max(float a, float b) {
  auto r = __builtin_amdgcn_permlane16_swap(f2u(a), f2u(b), false, false);
  return vmax(u2f(r[0]), u2f(r[1]));
}
__device__ __forceinline__ float xrow_max(float x) { x = swap16_max(x, x); return swap32_max(x, x); }
__device__ __forceinline__ float xrow_sum(float x) { x = swap16_add(x, x); return swap32_add(x, x); }
__device__ __forceinline__ float row_max(float x) {
  x = vmax(x, dppf<ROR8>(x)); x = vmax(x, dppf<ROR4>(x)); x = vmax(x, dppf<ROR2>(x)); return vmax(x, dppf<ROR1>(x));
}
__device__ __forceinline__ float row_sum(float x) {
  x += dppf<ROR8>(x); x += dppf<ROR4>(x); x += dppf<ROR2>(x); return x + dppf<ROR1>(x);
}
__device__ __forceinline__ float rdlane(float x, int l) { return u2f(__builtin_amdgcn_readlane(f2u(x), l)); }
#define LD_AUX 16
#define ST_AUX 16
__device__ __forceinline__ v4u ldu(rsrc_t rs, int off) { return __builtin_amdgcn_raw_buffer_load_b128(rs, off, 0, LD_AUX); }
__device__ __forceinline__ void stu(rsrc_t rs, int off, unsigned v0, unsigned v1, unsigned tag) {
  v4u u = {v0, tag, v1, tag};
  __builtin_amdgcn_raw_buffer_store_b128(u, rs, off, 0, ST_AUX);
}
__device__ __forceinline__ bool uok(v4u u, unsigned tag) { return ((u.y ^ tag) | (u.w ^ tag)) < 256u; }

__device__ __forceinline__ unsigned load_tag(const unsigned char* ws) {
  unsigned e;
  asm volatile("s_load_dword %0, %1, 0x0\n\ts_waitcnt lgkmcnt(0)" : "=s"(e) : "s"(ws) : "memory");
  SB();
  return (e + 256u) & ~255u;
}

#define PSLEEP 2
#define POLL_SLEEP() do { if (PSLEEP) __builtin_amdgcn_s_sleep(PSLEEP); } while (0)
struct Spin {
  unsigned n = 0;
  __device__ __forceinline__ bool expired() { return ++n > (1u << 21); }
};
template <int SLEEP> __device__ __forceinline__ void lds_wait4(LAS unsigned char* L, int grp) {
  Spin sp;
  for (;;) {
    const v4u f = *(volatile LAS v4u*)(L + L_FLAG + grp * 4);
    if (__builtin_amdgcn_readfirstlane(f.x & f.y & f.z & f.w) != 0u) break;
    if (sp.expired()) break;
    if (SLEEP) __builtin_amdgcn_s_sleep(SLEEP);
  }
  asm volatile("" ::: "memory");
}
template <int SLEEP> __device__ __forceinline__ void lds_wait1(LAS unsigned char* L, int word) {
  Spin sp;
  for (;;) {
    const unsigned f = *(volatile LAS unsigned*)(L + L_FLAG + word * 4);
    if (__builtin_amdgcn_readfirstlane(f) != 0u) break;
    if (sp.expired()) break;
    if (SLEEP) __builtin_amdgcn_s_sleep(SLEEP);
  }
  asm volatile("" ::: "memory");
}
__device__ __forceinline__ void lds_set(LAS unsigned char* L, int word) {
  asm volatile("" ::: "memory");
  *(volatile LAS unsigned*)(L + L_FLAG + word * 4) = 1u;
}
__device__ __forceinline__ void delta_sel(int jj, unsigned sb, unsigned& s0, unsigned& s1) {
  const unsigned pr = sb | ((sb + 1u) << 8);
  const unsigned lo = 0x0c0c0000u | pr, hi = (pr << 16) | 0x0c0cu, z = 0x0c0c0c0cu;
  s0 = jj == 0 ? lo : (jj == 1 ? hi : z);
  s1 = jj == 2 ? lo : (jj == 3 ? hi : z);
}
#define PERM(x, s) __builtin_amdgcn_perm(0u, (x), (s))

#define STAMP(k) do { } while (0)

#define TQ 315
#define TQ2 70
#define WARM_INBOX 1
#define WARM_AUX 16
#define CODE_PF 0
#define TW 0
#define OUT_AUX -1
#define NRED 64
#define PRIO 1
#define VSYNC 1
#define TPD2 6
#define TPD 12
__device__ __forceinline__ void wait_until(unsigned long long t) {
  while ((long long)(__builtin_amdgcn_s_memrealtime() - t) < 0) __builtin_amdgcn_s_sleep(1);
}
#define REPOLL(u, off) do { const bool ok_ = uok(u, tag); const v4u n_ = ldu(rs, ok_ ? OOB : (off)); u = ok_ ? u : n_; } while (0)

#define N1 8
#define LDS_WAIT_WITH(grp, BODY) do { Spin sp_; for (;;) { \
    asm volatile("" ::: "memory"); \
    const v4u f_ = *(volatile LAS v4u*)(L + L_FLAG + (grp) * 4); \
    BODY; \
    if (__builtin_amdgcn_readfirstlane(f_.x & f_.y & f_.z & f_.w) != 0u) break; \
    if (sp_.expired()) break; } } while (0)
#define LDSV(T, off) (*(volatile LAS T*)(L + (off)))

__global__ void __launch_bounds__(768, 3) fused_kernel(Params p) {
  __shared__ __attribute__((aligned(16))) unsigned char lds_raw[L_BYTES];
  LAS unsigned char* L = (LAS unsigned char*)lds_raw;
  const int tid = threadIdx.x, lane = tid & 63, wave = __builtin_amdgcn_readfirstlane(tid >> 6);
  const int blk = blockIdx.x, xg = blk & 7, xj = blk >> 3;
  const int n16 = lane & 15, a4 = lane >> 4, w = wave & 3;
  const int role = wave < 4 ? 2 : (wave >> 3);
  const int c8 = lane & 7, r0 = (lane >> 3) & 1, rho = lane >> 3;
  const int abb = (n16 >> 2) & 1;
  const int ab = xj >> 4, as = xj & 15;
  const bool hasF = xj < 28;
  const int rb = xg * 16 + (xj >> 1), ch = xj & 1, fgrp = xg * 14 + (xj >> 1);
  STAMP(0);
  v4u dr[16], xa, xb;
  *(LAS unsigned*)(L + L_FLAG + lane * 4) = 0u;
  const unsigned long long tstart = __builtin_amdgcn_s_memrealtime();
  const bool isq = wave == 0;
  const int ks = xg >> 1, cb = 2 * xj + ((xg ^ xj) & 1);
  const unsigned wlo = (((unsigned)(ab * 8 + xg) << 23) | 0x700000u);
  const int wofs = (4 * as + w) * 1024;
  const int va = ((c8 * (isq ? 16 : 0x11000)) ^ (isq ? 0 : wofs)) + (r0 << (isq ? 17 : 6)) + (a4 << (isq ? 18 : 4));
  if (isq || role == 0) {
    const int sstr = isq ? 8192 : 128;
    int so[8];
    so[0] = isq ? (ks << 20) + cb * 128 : 0;
#pragma unroll
    for (int u = 1; u < 8; ++u) { so[u] = so[u - 1] + sstr; asm volatile("" : "+s"(so[u])); }
    const rsrc_t r1 = mkrs(isq ? p.wq : p.k + wlo, 512 * 8192);
    asm volatile("s_waitcnt lgkmcnt(0)" :: "v"(va) : "memory");
    SB();
    if (isq) {
      const int xo = (abb * 512 + 128 * ks + 16 * rho) * 2;
      xa = *(const GAS v4u*)(p.x + xo);
      xb = *(const GAS v4u*)(p.x + xo + 16);
    } else {
      __builtin_amdgcn_s_barrier();
    }
    STAMP(9);
    SB();
#pragma unroll
    for (int u = 0; u < 8; ++u) { dr[u] = ldnt(r1, va, so[u]); SB(); }
    const int vb = va ^ (isq ? 65536 : 0x88040);
    if (!isq) lds_wait1<1>(L, F_QP);
#pragma unroll
    for (int u = 0; u < 8; ++u) { dr[8 + u] = ldnt(r1, vb, so[u]); SB(); }
    if (!isq) lds_set(L, F_KISS + w);
    STAMP(1);
    if (isq) {
      __builtin_amdgcn_s_barrier();
      SB();
      __builtin_amdgcn_s_setprio(3);
      const unsigned ep = *(const __attribute__((address_space(4))) unsigned*)(unsigned long long)p.ws;
      SB();
      unsigned e0, e1, o0, o1;
      delta_sel(n16 & 3, 0u, e0, e1);
      delta_sel(n16 & 3, 2u, o0, o1);
      const int off = WS_Q + ((((cb >> 3) * 2 + (a4 & 1)) * 4 + ks) * 128 + (cb & 7) * 16 + (a4 >> 1) * 8 + n16) * 16;
      f32x4 d0 = {0.f, 0.f, 0.f, 0.f}, d1 = {0.f, 0.f, 0.f, 0.f};
#pragma unroll
      for (int u = 0; u < 16; ++u) {
        const unsigned xs = (u < 8) ? xa[(u >> 1) & 3] : xb[(u >> 1) & 3];
        const unsigned A0 = PERM(xs, (u & 1) ? o0 : e0), A1 = PERM(xs, (u & 1) ? o1 : e1);
        const v4u wv = dr[u];
        d0 = mfma16(A0, A1, wv.x, wv.y, d0);
        d1 = mfma16(A0, A1, wv.z, wv.w, d1);
      }
      float sv[4];
#pragma unroll
      for (int i = 0; i < 4; ++i) sv[i] = swap32_add(dppf_rows<ROR8, 0xc>(d0[i]), dppf_rows<ROR8, 0xc>(d1[i]));
      stu(mkrs(p.ws, WS_RSRC_BYTES), n16 < 8 ? off : OOB, pkbf(sv[0], sv[1]), pkbf(sv[2], sv[3]), (ep + 256u) & ~255u);
      SB();
      lds_set(L, F_QP);
      __builtin_amdgcn_s_setprio(0);
      STAMP(13);
    }
  } else {
    asm volatile("s_waitcnt lgkmcnt(0)" ::: "memory");
    __builtin_amdgcn_s_barrier();
  }
  SB();
  const unsigned epoch = *(const __attribute__((address_space(4))) unsigned*)(unsigned long long)p.ws;
  SB();
  unsigned sE0, sE1, sO0, sO1;
  delta_sel(n16 & 3, 0u, sE0, sE1);
  delta_sel(n16 & 3, 2u, sO0, sO1);
  SB();
  const rsrc_t rs = mkrs(p.ws, WS_RSRC_BYTES);
  const unsigned tag = (epoch + 256u) & ~255u;

  if (role == 2) {
    {
      const rsrc_t grs_ = mkrs(p.x, w == 1 ? 2048 : 0), gwrs = mkrs(p.gw, w == 1 ? 1024 : 0);
      const int wbase = w == 1 ? (blk < 64 ? WS_R + blk * 8192 : OOB) : (w == 2 ? WS_O + rb * 4096 : (hasF ? WS_F + fgrp * 4096 : OOB));
      const v4u ga = __builtin_amdgcn_raw_buffer_load_b128(grs_, lane * 16, 0, 0);
      const v4u gb = __builtin_amdgcn_raw_buffer_load_b128(grs_, lane * 16 + 1024, 0, 0);
      const v4u gw = __builtin_amdgcn_raw_buffer_load_b128(gwrs, lane * 16, 0, 0);
      const unsigned wv = __builtin_amdgcn_raw_buffer_load_b32(rs, (WARM_INBOX == 1 && w != 0 && (w == 1 || lane < 32)) ? wbase + lane * 128 : OOB, 0, 16);
      SB();
      asm volatile("" :: "s"(tag));
      lds_set(L, F_FT + w);
      SB();
      asm volatile("" :: "v"(wv), "v"(ga), "v"(gb), "v"(gw), "v"(dr[15]));
      STAMP(15);
      if (w == 1) {
        float s0 = 0.f, s1 = 0.f;
#pragma unroll
        for (int i = 0; i < 4; ++i) {
          s0 = fmaf(bflo(ga[i]), bflo(gw[i]), s0); s0 = fmaf(bfhi(ga[i]), bfhi(gw[i]), s0);
          s1 = fmaf(bflo(gb[i]), bflo(gw[i]), s1); s1 = fmaf(bfhi(gb[i]), bfhi(gw[i]), s1);
        }
        float sg = swap32_add(s0, s1);
        sg = row_sum(swap16_add(sg, sg));
        const float gt = 1.f / (1.f + __expf(-sg));
        if ((lane & 31) == 0) *(LAS float*)(L + L_GT + (lane >> 5) * 4) = gt;
        lds_set(L, F_G);
      }
    }
    const rsrc_t drs = mkrs(p.wd, hasF ? 512 * 3584 : 0), ors = mkrs(p.wo, 4096 * 1024);
    const int co = ch * 512 + w * 128 + c8 * 16;
    const int dso = ((fgrp >> 2) * 64 + (fgrp & 1) * 32 + ((fgrp >> 1) & 1) * 4) * 1024;
    const int dvo = ((rho >> 1) * 8 + (rho & 1) * 2) * 1024 + co;
    int roff = NRED == 64 ? WS_R + ((((ch * 4 + w) * 4 + a4) * 2 + (c8 >> 2)) * 128 + rb) * 64 + (c8 & 3) * 16
                          : WS_R + (((ch * 4 + w) * 4 + a4) * 128 + rb) * 128 + c8 * 16;
    roff = n16 < 8 ? roff : OOB;
    asm volatile("" : "+v"(roff));
    SB();
    lds_wait4<1>(L, F_VISS);
    if (TW) wait_until(tstart + TW);
    v4u wdr[2], wor[4];
#pragma unroll
    for (int u = 0; u < 2; ++u) { wdr[u] = ldnt(drs, dvo, dso + u * 1024); SB(); }
#pragma unroll
    for (int u = 0; u < 4; ++u) { wor[u] = ldnt(ors, rho * 4096 + co, rb * 32768 + u * 1024); SB(); }
    STAMP(2);
    asm volatile("" :: "v"(wor[3]));
    STAMP(3);
    v4u av;
    LDS_WAIT_WITH(F_AV, av = LDSV(v4u, L_AV + (abb * 8 + rho) * 16));
    if (PRIO) __builtin_amdgcn_s_setprio(3);
    STAMP(7);
    {
      f32x4 d0 = {0.f, 0.f, 0.f, 0.f}, d1 = {0.f, 0.f, 0.f, 0.f}, d2 = {0.f, 0.f, 0.f, 0.f}, d3 = {0.f, 0.f, 0.f, 0.f};
#pragma unroll
      for (int u = 0; u < 6; ++u) {
        const unsigned xs = av[u >> 1];
        const unsigned A0 = PERM(xs, (u & 1) ? sO0 : sE0), A1 = PERM(xs, (u & 1) ? sO1 : sE1);
        const v4u wv = u < 4 ? wor[u & 3] : wdr[u & 1];
        if (u & 1) { d2 = mfma16(A0, A1, wv.x, wv.y, d2); d3 = mfma16(A0, A1, wv.z, wv.w, d3); }
        else { d0 = mfma16(A0, A1, wv.x, wv.y, d0); d1 = mfma16(A0, A1, wv.z, wv.w, d1); }
      }
      d0 += d2; d1 += d3;
      float sv[4];
#pragma unroll
      for (int i = 0; i < 4; ++i) sv[i] = swap32_add(dppf_rows<ROR8, 0xc>(d0[i]), dppf_rows<ROR8, 0xc>(d1[i]));
      stu(rs, roff, pkbf(sv[0], sv[1]), pkbf(sv[2], sv[3]), tag);
    }
    SB();
    lds_set(L, F_PUB + w);
    STAMP(8);
  } else if (role == 1) {
    if (PRIO) __builtin_amdgcn_s_setprio(1);
    const int frow = 64 * xg + 32 * (w >> 1) + 4 * rho;
    int ooff = lane < 32 ? WS_O + (((xg * 16 + w * 4 + (rho & 3)) * 32 + xj) * 8 + c8) * 16 : OOB;
    const v2u xf = *(const GAS v2u*)(p.x + (abb * 512 + frow) * 2);
    v4u vr[16], gu[4];
    {
      const rsrc_t vrs = mkrs(p.v + wlo, 1 << 20);
      lds_wait4<1>(L, F_KISS);
      STAMP(9);
#pragma unroll
      for (int u = 0; u < 16; ++u) { vr[u] = ldnt(vrs, lane * 16, wofs ^ (u * 0x11000)); SB(); }
      lds_set(L, F_GU + w);
      STAMP(1);
    }
    {
      const rsrc_t grs = mkrs((w & 1) ? p.wu : p.wg, hasF ? 512 * 3584 : 0);
      const int go = frow * 3584 + xj * 128 + c8 * 16;
      if (VSYNC) lds_wait4<1>(L, F_GU);
#pragma unroll
      for (int u = 0; u < 4; ++u) { gu[u] = ldnt(grs, go, u * 3584); SB(); }
    }
    lds_set(L, F_VISS + w);
    STAMP(2);
    int foff = WS_F + (xj * 4 + (a4 >> 1) * 2 + (c8 >> 2)) * 4096 + ((((a4 & 1) * 2 + (w & 1)) * 16 + xg * 2 + (w >> 1)) * 4 + (c8 & 3)) * 16;
    foff = (hasF && n16 < 8) ? foff : OOB;
    asm volatile("" : "+v"(ooff), "+v"(foff) : "s"(tag));
    lds_wait4<1>(L, F_FT);
    v4u pa, pb;
    {
      Spin sp;
      for (;;) {
        asm volatile("" ::: "memory");
        const unsigned f = LDSV(unsigned, L_FLAG + (F_P + w) * 4);
        pa = LDSV(v4u, L_P + w * 512 + (n16 >> 2) * 128 + a4 * 32); pb = LDSV(v4u, L_P + w * 512 + (n16 >> 2) * 128 + a4 * 32 + 16);
        if (__builtin_amdgcn_readfirstlane(f) != 0u) break;
        if (sp.expired()) break;
        __builtin_amdgcn_s_sleep(1);
      }
    }
    STAMP(3);
    f32x4 o0 = {0.f, 0.f, 0.f, 0.f}, o1 = {0.f, 0.f, 0.f, 0.f}, o2 = {0.f, 0.f, 0.f, 0.f}, o3 = {0.f, 0.f, 0.f, 0.f};
    float fsc;
    unsigned otag0, otag1;
#pragma unroll
    for (int t = 0; t < 16; ++t) {
      const unsigned ps = (t < 8) ? pa[(t >> 1) & 3] : pb[(t >> 1) & 3];
      const unsigned A0 = PERM(ps, (t & 1) ? sO0 : sE0), A1 = PERM(ps, (t & 1) ? sO1 : sE1);
      if (t & 1) { o2 = mfma16(A0, A1, vr[t].x, vr[t].y, o2); o3 = mfma16(A0, A1, vr[t].z, vr[t].w, o3); }
      else { o0 = mfma16(A0, A1, vr[t].x, vr[t].y, o0); o1 = mfma16(A0, A1, vr[t].z, vr[t].w, o1); }
      if (t == 9) {
        f32x4 m, l;
        LDS_WAIT_WITH(F_L, m = LDSV(f32x4, L_MX + a4 * 16); l = LDSV(f32x4, L_LS + a4 * 16));
        const float M = vmax(vmax(m[0], m[1]), vmax(m[2], m[3]));
        const float e0 = __builtin_amdgcn_exp2f(m[0] - M), e1 = __builtin_amdgcn_exp2f(m[1] - M), e2 = __builtin_amdgcn_exp2f(m[2] - M), e3 = __builtin_amdgcn_exp2f(m[3] - M);
        const float ls = (e0 * l[0] + e1 * l[1]) + (e2 * l[2] + e3 * l[3]);
        fsc = (w == 0 ? e0 : (w == 1 ? e1 : (w == 2 ? e2 : e3))) * __builtin_amdgcn_rcpf(ls);
        const unsigned lse = __builtin_amdgcn_readlane(f2u(M + __builtin_amdgcn_logf(ls)), w * 16);
        const unsigned h16 = (lane & 1) ? lse : lse >> 16;
        otag0 = tag | (h16 & 255u); otag1 = tag | ((h16 >> 8) & 255u);
      }
    }
    *(LAS f32x4*)(L + L_OM + (w * 512 + a4 * 128 + n16 * 8) * 4) = (o0 + o2) * fsc;
    *(LAS f32x4*)(L + L_OM + (w * 512 + a4 * 128 + n16 * 8 + 4) * 4) = (o1 + o3) * fsc;
    lds_set(L, F_O + w);
    STAMP(4);
    {
      const int ob = L_OM + ((lane >> 5) * 1024 + w * 128 + (lane & 31) * 4) * 4;
      f32x4 sa, sb;
      LDS_WAIT_WITH(F_O, sa = LDSV(f32x4, ob); sb = LDSV(f32x4, ob + 2048));
      const f32x4 s2 = sa + sb;
      const float t0 = swap32_add(s2[0], s2[2]), t1 = swap32_add(s2[1], s2[3]);
      const unsigned pk = pkbf(t0, t1);
      const auto sw = __builtin_amdgcn_permlane32_swap(pk, pk, false, false);
      const v4u u = {sw[0], otag0, sw[1], otag1};
      __builtin_amdgcn_raw_buffer_store_b128(u, rs, ooff, 0, ST_AUX);
    }
    SB();
    STAMP(5);
    {
      f32x4 d0 = {0.f, 0.f, 0.f, 0.f}, d1 = {0.f, 0.f, 0.f, 0.f};
#pragma unroll
      for (int u = 0; u < 4; ++u) {
        const unsigned xs = xf[u >> 1];
        const unsigned A0 = PERM(xs, (u & 1) ? sO0 : sE0), A1 = PERM(xs, (u & 1) ? sO1 : sE1);
        d0 = mfma16(A0, A1, gu[u].x, gu[u].y, d0);
        d1 = mfma16(A0, A1, gu[u].z, gu[u].w, d1);
      }
      float sv[4];
#pragma unroll
      for (int i = 0; i < 4; ++i) sv[i] = swap32_add(dppf_rows<ROR8, 0xc>(d0[i]), dppf_rows<ROR8, 0xc>(d1[i]));
      stu(rs, foff, pkbf(sv[0], sv[1]), pkbf(sv[2], sv[3]), tag);
    }
    SB();
    lds_set(L, F_FP + w);
    STAMP(6);
    if (blk < NRED) lds_wait4<1>(L, F_PUB);
  } else {
    if (PRIO) __builtin_amdgcn_s_setprio(2);
    const int qoff = WS_Q + (((xg * 2 + ab) * 4 + 2 * (lane >> 5)) * 128 + w * 32 + (lane & 31)) * 16;
    const int pos = lane & 31;
    int qw = L_QS + (w * 128 + (pos >> 4) * 64 + (pos & 7) * 8 + ((pos >> 3) & 1) * 4 + (lane >> 5) * 2) * 2;
    const int qa = L_QS + ((n16 & 3) * 128 + ((n16 >> 2) & 1) * 32 + a4 * 8) * 2;
    int qa0o = n16 < 8 ? qa : L_ZERO, qa1o = n16 < 8 ? qa + 128 : L_ZERO, qc0o = n16 < 8 ? L_ZERO : qa, qc1o = n16 < 8 ? L_ZERO : qa + 128;
    asm volatile("" : "+v"(qw), "+v"(qa0o), "+v"(qa1o), "+v"(qc0o), "+v"(qc1o));
    wait_until(tstart + TQ);
    v4u q0 = ldu(rs, qoff), q1 = ldu(rs, qoff + 2048);
    SB();
    STAMP(2);
    {
      Spin sp;
#pragma unroll 1
      while (!__all(uok(q0, tag) && uok(q1, tag))) {
        asm volatile("" ::: "memory");
        REPOLL(q0, qoff); REPOLL(q1, qoff + 2048);
        if (sp.expired()) break;
      }
    }
    STAMP(4);
    {
      constexpr float QS = 0.088388347648318440f * 1.4426950408889634f;
      const float c0 = bflo(q0.x) + bflo(q1.x), c1 = bfhi(q0.x) + bfhi(q1.x), c2 = bflo(q0.z) + bflo(q1.z), c3 = bfhi(q0.z) + bfhi(q1.z);
      const float e0 = swap32_add(c0, c2) * QS, e1 = swap32_add(c1, c3) * QS;
      *(LAS unsigned*)(L + qw) = pkbf(e0, e1);
    }
    lds_set(L, F_Q + w);
    v4u qa0, qa1, qc0, qc1;
    LDS_WAIT_WITH(F_Q, qa0 = LDSV(v4u, qa0o); qa1 = LDSV(v4u, qa1o); qc0 = LDSV(v4u, qc0o); qc1 = LDSV(v4u, qc1o));
    f32x4 acc[2][2];
#pragma unroll
    for (int e = 0; e < 2; ++e)
#pragma unroll
      for (int P = 0; P < 2; ++P) acc[e][P] = mfma32(qa0, dr[e * 8 + P * 2], f32x4{0.f, 0.f, 0.f, 0.f});
#pragma unroll
    for (int e = 0; e < 2; ++e)
#pragma unroll
      for (int P = 0; P < 2; ++P) acc[e][P] = mfma32(qa1, dr[e * 8 + P * 2 + 1], acc[e][P]);
#pragma unroll
    for (int e = 0; e < 2; ++e)
#pragma unroll
      for (int P = 0; P < 2; ++P) acc[e][P] = mfma32(qc0, dr[e * 8 + P * 2 + 4], acc[e][P]);
#pragma unroll
    for (int e = 0; e < 2; ++e)
#pragma unroll
      for (int P = 0; P < 2; ++P) acc[e][P] = mfma32(qc1, dr[e * 8 + P * 2 + 5], acc[e][P]);
    const bool sel0 = ((a4 & 1) == (n16 >> 3));
    float S[4];
#pragma unroll
    for (int i = 0; i < 4; ++i) {
      const float y0 = dppf_rows<ROR8, 0xa>(sel0 ? acc[0][0][i] : acc[1][0][i]);
      const float y1 = dppf_rows<ROR8, 0xa>(sel0 ? acc[0][1][i] : acc[1][1][i]);
      S[i] = swap16_add(y0, y1);
    }
    const float mrow = row_max(swap16_max(swap32_max(S[0], S[1]), swap32_max(S[2], S[3])));
    const float m0 = rdlane(mrow, 0), m2 = rdlane(mrow, 16), m1 = rdlane(mrow, 32), m3 = rdlane(mrow, 48);
    const unsigned P01 = pkbf(__builtin_amdgcn_exp2f(S[0] - m0), __builtin_amdgcn_exp2f(S[1] - m1));
    const unsigned P23 = pkbf(__builtin_amdgcn_exp2f(S[2] - m2), __builtin_amdgcn_exp2f(S[3] - m3));
    {
      LAS unsigned short* pp = (LAS unsigned short*)(L + L_P + w * 512 + lane * 2);
      pp[0] = (unsigned short)P01; pp[64] = (unsigned short)(P01 >> 16); pp[128] = (unsigned short)P23; pp[192] = (unsigned short)(P23 >> 16);
      lds_set(L, F_P + w);
      SB();
      const float lrow = row_sum(swap16_add(swap32_add(bflo(P01), bfhi(P01)), swap32_add(bflo(P23), bfhi(P23))));
      const int hrow = ((a4 & 1) << 1) | (a4 >> 1);
      if (n16 == 0) { *(LAS float*)(L + L_MX + hrow * 16 + w * 4) = mrow; *(LAS float*)(L + L_LS + hrow * 16 + w * 4) = lrow; }
    }
    lds_set(L, F_L + w);
    STAMP(5);
    const int bb = w >> 1;
    lds_wait1<1>(L, F_G);
    const float gate = *(LAS float*)(L + L_GT + bb * 4);
    const bool isO = (w & 1) == 0;
    int goff = isO ? WS_O + rb * 4096 + bb * 2048 + lane * 16 : (hasF ? WS_F + fgrp * 4096 + bb * 2048 + lane * 16 : OOB);
    int vslot = L_AV + (isO ? (bb * 8 + c8) * 16 + a4 * 2 : (bb * 8 + (lane & 3) * 2 + (a4 >> 1)) * 16 + (4 + (a4 & 1)) * 2);
    asm volatile("" : "+v"(goff), "+v"(vslot));
    lds_wait4<1>(L, F_FP);
    __builtin_amdgcn_s_sleep(TPD);
    STAMP(14);
    v4u u0 = ldu(rs, goff), u1 = ldu(rs, goff + 1024);
    if (isO || hasF) {
      Spin sp;
#pragma unroll 1
      while (!__all(uok(u0, tag) && uok(u1, tag))) {
        asm volatile("" ::: "memory");
        REPOLL(u0, goff); REPOLL(u1, goff + 1024);
        if (sp.expired()) break;
        POLL_SLEEP();
      }
    }
    STAMP(8);
    float val;
    if (isO) {
      const unsigned hm = __builtin_amdgcn_perm(__builtin_amdgcn_perm(u1.w, u1.y, 0x0c0c0400u), __builtin_amdgcn_perm(u0.w, u0.y, 0x0c0c0400u), 0x05040100u);
      const unsigned ho = (unsigned)__builtin_amdgcn_mov_dpp((int)hm, 0xB1, 0xf, 0xf, true);
      const unsigned hi = (lane & 1) ? ho : hm, lo = (lane & 1) ? hm : ho;
      const float l0 = u2f(__builtin_amdgcn_perm(hi, lo, 0x05040100u)), l1 = u2f(__builtin_amdgcn_perm(hi, lo, 0x07060302u));
      float mm = vmax(l0, l1);
      mm = vmax(mm, dppf<ROR8>(mm)); mm = xrow_max(mm);
      const float w0 = __builtin_amdgcn_exp2f(l0 - mm), w1 = __builtin_amdgcn_exp2f(l1 - mm);
      float ll = w0 + w1;
      ll += dppf<ROR8>(ll); ll = xrow_sum(ll);
      const float sc = gate * __builtin_amdgcn_rcpf(ll);
      const float v0 = w0 * bflo(u0.x) + w1 * bflo(u1.x), v1 = w0 * bfhi(u0.x) + w1 * bfhi(u1.x);
      const float v2 = w0 * bflo(u0.z) + w1 * bflo(u1.z), v3 = w0 * bfhi(u0.z) + w1 * bfhi(u1.z);
      val = swap16_add(swap32_add(v0, v2), swap32_add(v1, v3));
      val += dppf<ROR8>(val);
      val *= sc;
    } else {
      float gs = swap16_add(swap32_add(bflo(u0.x), bflo(u0.z)), swap32_add(bfhi(u0.x), bfhi(u0.z)));
      float us = swap16_add(swap32_add(bflo(u1.x), bflo(u1.z)), swap32_add(bfhi(u1.x), bfhi(u1.z)));
      gs += dppf<ROR8>(gs); us += dppf<ROR8>(us);
      gs += dppf<ROR4>(gs); us += dppf<ROR4>(us);
      val = gs * __builtin_amdgcn_rcpf(1.f + __expf(-gs)) * us * (1.f - gate);
    }
    *(LAS unsigned short*)(L + vslot) = (unsigned short)pkbf(val, val);
    lds_set(L, F_AV + w);
    STAMP(6);
    if (blk < NRED) lds_wait4<1>(L, F_PUB);
  }
  if (blk < NRED) {
    if (role < 2) {
      const int roff = WS_R + blk * (NRED == 64 ? 8192 : 16384) + (tid - 256) * 16;
      __builtin_amdgcn_s_sleep(TPD2);
      v4u u = ldu(rs, roff), u2 = u;
      if (NRED == 32) u2 = ldu(rs, roff + 8192);
      Spin sp;
#pragma unroll 1
      while (!__all(uok(u, tag) && uok(u2, tag))) {
        asm volatile("" ::: "memory");
        REPOLL(u, roff);
        if (NRED == 32) REPOLL(u2, roff + 8192); else u2 = u;
        if (sp.expired()) break;
        POLL_SLEEP();
      }
      STAMP(10);
      if (NRED == 64) {
        float vv = swap16_add(swap32_add(bflo(u.x), bflo(u.z)), swap32_add(bfhi(u.x), bfhi(u.z)));
        vv += dppf<ROR8>(vv); vv += dppf<ROR4>(vv);
        if ((lane & 12) == 0) *(LAS float*)(L + L_RED + (((lane & 3) * 4 + a4) * 8 + wave - 4) * 4) = vv;
      } else {
        float vv = swap16_add(swap32_add(bflo(u.x) + bflo(u2.x), bflo(u.z) + bflo(u2.z)), swap32_add(bfhi(u.x) + bfhi(u2.x), bfhi(u.z) + bfhi(u2.z)));
        vv += dppf<ROR8>(vv);
        if ((lane & 8) == 0) *(LAS float*)(L + L_RED + ((c8 * 4 + a4) * 8 + wave - 4) * 4) = vv;
      }
    }
    __syncthreads();
    if (tid < 1024 / NRED) {
      const f32x4 s0 = *(LAS f32x4*)(L + L_RED + tid * 32), s1 = *(LAS f32x4*)(L + L_RED + tid * 32 + 16);
      const float sum = ((s0[0] + s0[1]) + (s0[2] + s0[3])) + ((s1[0] + s1[1]) + (s1[2] + s1[3]));
      const int col = NRED == 64 ? (blk >> 3) * 64 + ((blk & 1) * 4 + (tid >> 2)) * 8 + ((blk >> 2) & 1) * 4 + (tid & 3)
                                 : (blk >> 2) * 64 + (tid >> 2) * 8 + ((blk >> 1) & 1) * 4 + (tid & 3);
      const int ob = NRED == 64 ? (blk >> 1) & 1 : blk & 1;
      *(GAS unsigned short*)(p.out + (ob * 512 + col) * 2) = (unsigned short)pkbf(sum, sum);
      if (blk == 0 && tid == 0) __hip_atomic_store((GAS unsigned*)(p.ws + WS_EPOCH), tag, RLX_AGENT);
    }
    STAMP(11);
  }
}

}

extern "C" void kernel_launch(void* const* d_in, const int* in_sizes, int n_in, void* d_out, int out_size, void* d_ws, size_t ws_size,
                              hipStream_t stream) {
  Params p;
  p.x = (const unsigned char*)d_in[0];
  p.k = (const unsigned char*)d_in[1];
  p.v = (const unsigned char*)d_in[2];
  p.wq = (const unsigned char*)d_in[3];
  p.wo = (const unsigned char*)d_in[4];
  p.wg = (const unsigned char*)d_in[6];
  p.wu = (const unsigned char*)d_in[7];
  p.wd = (const unsigned char*)d_in[8];
  p.gw = (const unsigned char*)d_in[9];
  p.out = (unsigned char*)d_out;
  p.ws = (unsigned char*)d_ws;
  hipLaunchKernelGGL(fused_kernel, dim3(256), dim3(768), 0, stream, p);
}
```

```cpp
#include <hip/hip_runtime.h>
#include <hip/hip_bf16.h>
#include <stdint.h>

#define GAS __attribute__((address_space(1)))
#define LAS __attribute__((address_space(3)))
#define RLX_AGENT __ATOMIC_RELAXED, __HIP_MEMORY_SCOPE_AGENT
#define SB() __builtin_amdgcn_sched_barrier(0)
typedef unsigned v4u __attribute__((ext_vector_type(4)));
typedef unsigned v2u __attribute__((ext_vector_type(2)));
typedef float f32x4 __attribute__((ext_vector_type(4)));
typedef __bf16 bf16x8 __attribute__((ext_vector_type(8)));
typedef __bf16 bf16x2 __attribute__((ext_vector_type(2)));
typedef short s16x4 __attribute__((ext_vector_type(4)));

namespace {

constexpr int WS_EPOCH = 0;
constexpr int WS_Q = 4096;
constexpr int WS_F = WS_Q + 8 * 2 * 4 * 128 * 16;
constexpr int WS_O = WS_F + 112 * 4096;
constexpr int WS_R = WS_O + 128 * 4096;
constexpr int WS_END = WS_R + 64 * 8192;
constexpr int WS_RSRC_BYTES = WS_END;
constexpr int OOB = 0x7fffff00;

constexpr int L_FLAG = 0;
constexpr int L_ZERO = 192;
constexpr int L_QS = 256;
constexpr int L_P = L_QS + 1024;
constexpr int L_MX = L_P + 2048;
constexpr int L_LS = L_MX + 64;
constexpr int L_AV = L_LS + 64;
constexpr int L_GT = L_AV + 256;
constexpr int L_RED = L_GT + 16;
constexpr int L_OM = L_RED + 512;
constexpr int L_BYTES = L_OM + 8192;
constexpr int F_KISS = 0, F_Q = 4, F_P = 8, F_L = 12, F_O = 16, F_AV = 20, F_PUB = 24, F_G = 28, F_QP = 29, F_FP = 32, F_VISS = 36, F_GU = 40, F_FT = 44;

struct Params {
  const unsigned char *x, *wq, *k, *v, *wo, *wg, *wu, *wd, *gw;
  unsigned char *out, *ws;
};

typedef __amdgpu_buffer_rsrc_t rsrc_t;
__device__ __forceinline__ rsrc_t mkrs(const unsigned char* base, int bytes) {
  return __builtin_amdgcn_make_buffer_rsrc((void*)base, 0, bytes, 0x00020000);
}
__device__ __forceinline__ v4u ldnt(rsrc_t r, int voff, int soff) { return __builtin_amdgcn_raw_buffer_load_b128(r, voff, soff, 2); }
__device__ __forceinline__ unsigned pkbf(float lo, float hi) {
  bf16x2 r = {(__bf16)lo, (__bf16)hi};
  return __builtin_bit_cast(unsigned, r);
}
__device__ __forceinline__ float bflo(unsigned u) { return __builtin_bit_cast(float, u << 16); }
__device__ __forceinline__ float bfhi(unsigned u) { return __builtin_bit_cast(float, u & 0xffff0000u); }
__device__ __forceinline__ float u2f(unsigned u) { return __builtin_bit_cast(float, u); }
__device__ __forceinline__ unsigned f2u(float f) { return __builtin_bit_cast(unsigned, f); }
__device__ __forceinline__ f32x4 mfma32(v4u a, v4u b, f32x4 c) {
  return __builtin_amdgcn_mfma_f32_16x16x32_bf16(__builtin_bit_cast(bf16x8, a), __builtin_bit_cast(bf16x8, b), c, 0, 0, 0);
}
__device__ __forceinline__ f32x4 mfma16(unsigned a0, unsigned a1, unsigned b0, unsigned b1, f32x4 c) {
  v2u a = {a0, a1}, b = {b0, b1};
  return __builtin_amdgcn_mfma_f32_16x16x16bf16_1k(__builtin_bit_cast(s16x4, a), __builtin_bit_cast(s16x4, b), c, 0, 0, 0);
}
template <int CTRL> __device__ __forceinline__ float dppf(float x) {
  return u2f(__builtin_amdgcn_mov_dpp((int)f2u(x), CTRL, 0xf, 0xf, true));
}
template <int CTRL, int ROWMASK> __device__ __forceinline__ float dppf_rows(float x) {
  return u2f(__builtin_amdgcn_update_dpp((int)f2u(x), (int)f2u(x), CTRL, ROWMASK, 0xf, false));
}
constexpr int ROR8 = 0x128, ROR4 = 0x124, ROR2 = 0x122, ROR1 = 0x121;
__device__ __forceinline__ float vmax(float a, float b) { return __builtin_amdgcn_fmed3f(a, b, 3.4028234e38f); }
__device__ __forceinline__ float swap32_add(float a, float b) {
  auto r = __builtin_amdgcn_permlane32_swap(f2u(a), f2u(b), false, false);
  return u2f(r[0]) + u2f(r[1]);
}
__device__ __forceinline__ float swap32_max(float a, float b) {
  auto r = __builtin_amdgcn_permlane32_swap(f2u(a), f2u(b), false, false);
  return vmax(u2f(r[0]), u2f(r[1]));
}
__device__ __forceinline__ float swap16_add(float a, float b) {
  auto r = __builtin_amdgcn_permlane16_swap(f2u(a), f2u(b), false, false);
  return u2f(r[0]) + u2f(r[1]);
}
__device__ __forceinline__ float swap16_max(float a, float b) {
  auto r = __builtin_amdgcn_permlane16_swap(f2u(a), f2u(b), false, false);
  return vmax(u2f(r[0]), u2f(r[1]));
}
__device__ __forceinline__ float xrow_max(float x) { x = swap16_max(x, x); return swap32_max(x, x); }
__device__ __forceinline__ float xrow_sum(float x) { x = swap16_add(x, x); return swap32_add(x, x); }
__device__ __forceinline__ float row_max(float x) {
  x = vmax(x, dppf<ROR8>(x)); x = vmax(x, dppf<ROR4>(x)); x = vmax(x, dppf<ROR2>(x)); return vmax(x, dppf<ROR1>(x));
}
__device__ __forceinline__ float row_sum(float x) {
  x += dppf<ROR8>(x); x += dppf<ROR4>(x); x += dppf<ROR2>(x); return x + dppf<ROR1>(x);
}
__device__ __forceinline__ float rdlane(float x, int l) { return u2f(__builtin_amdgcn_readlane(f2u(x), l)); }
constexpr int SC1 = 16;
__device__ __forceinline__ v4u ldu(rsrc_t rs, int off) { return __builtin_amdgcn_raw_buffer_load_b128(rs, off, 0, SC1); }
__device__ __forceinline__ void stu(rsrc_t rs, int off, unsigned v0, unsigned v1, unsigned tag) {
  v4u u = {v0, tag, v1, tag};
  __builtin_amdgcn_raw_buffer_store_b128(u, rs, off, 0, SC1);
}
__device__ __forceinline__ bool uok(v4u u, unsigned tag) { return ((u.y ^ tag) | (u.w ^ tag)) < 256u; }

template <int LOG2> struct SpinT {
  unsigned n = 0;
  __device__ __forceinline__ bool expired() { return ++n > (1u << LOG2); }
};
typedef SpinT<25> Spin;
typedef SpinT<21> SpinG;
template <int SLEEP> __device__ __forceinline__ void lds_wait4(LAS unsigned char* L, int grp) {
  Spin sp;
  for (;;) {
    const v4u f = *(volatile LAS v4u*)(L + L_FLAG + grp * 4);
    if (__builtin_amdgcn_readfirstlane(f.x & f.y & f.z & f.w) != 0u) break;
    if (sp.expired()) break;
    if (SLEEP) __builtin_amdgcn_s_sleep(SLEEP);
  }
  asm volatile("" ::: "memory");
}
template <int SLEEP> __device__ __forceinline__ void lds_wait1(LAS unsigned char* L, int word) {
  Spin sp;
  for (;;) {
    const unsigned f = *(volatile LAS unsigned*)(L + L_FLAG + word * 4);
    if (__builtin_amdgcn_readfirstlane(f) != 0u) break;
    if (sp.expired()) break;
    if (SLEEP) __builtin_amdgcn_s_sleep(SLEEP);
  }
  asm volatile("" ::: "memory");
}
__device__ __forceinline__ void lds_set(LAS unsigned char* L, int word) {
  asm volatile("" ::: "memory");
  *(volatile LAS unsigned*)(L + L_FLAG + word * 4) = 1u;
}
__device__ __forceinline__ void delta_sel(int jj, unsigned sb, unsigned& s0, unsigned& s1) {
  const unsigned pr = sb | ((sb + 1u) << 8);
  const unsigned lo = 0x0c0c0000u | pr, hi = (pr << 16) | 0x0c0cu, z = 0x0c0c0c0cu;
  s0 = jj == 0 ? lo : (jj == 1 ? hi : z);
  s1 = jj == 2 ? lo : (jj == 3 ? hi : z);
}
#define PERM(x, s) __builtin_amdgcn_perm(0u, (x), (s))

constexpr int TQ = 315;
constexpr int TPD = 12;
constexpr int TPD2 = 6;
__device__ __forceinline__ void wait_until(unsigned long long t) {
  while ((long long)(__builtin_amdgcn_s_memrealtime() - t) < 0) __builtin_amdgcn_s_sleep(1);
}
#define REPOLL(u, off) do { const bool ok_ = uok(u, tag); const v4u n_ = ldu(rs, ok_ ? OOB : (off)); u = ok_ ? u : n_; } while (0)

#define LDS_WAIT_WITH(grp, BODY) do { Spin sp_; for (;;) { \
    asm volatile("" ::: "memory"); \
    const v4u f_ = *(volatile LAS v4u*)(L + L_FLAG + (grp) * 4); \
    BODY; \
    if (__builtin_amdgcn_readfirstlane(f_.x & f_.y & f_.z & f_.w) != 0u) break; \
    if (sp_.expired()) break; } } while (0)
#define LDSV(T, off) (*(volatile LAS T*)(L + (off)))

__global__ void __launch_bounds__(768, 3) fused_kernel(Params p) {
  __shared__ __attribute__((aligned(16))) unsigned char lds_raw[L_BYTES];
  LAS unsigned char* L = (LAS unsigned char*)lds_raw;
  const int tid = threadIdx.x, lane = tid & 63, wave = __builtin_amdgcn_readfirstlane(tid >> 6);
  const int blk = blockIdx.x, xg = blk & 7, xj = blk >> 3;
  const int n16 = lane & 15, a4 = lane >> 4, w = wave & 3;
  const int role = wave < 4 ? 2 : (wave >> 3);
  const int c8 = lane & 7, r0 = (lane >> 3) & 1, rho = lane >> 3;
  const int abb = (n16 >> 2) & 1;
  const int ab = xj >> 4, as = xj & 15;
  const int fid = (xg & 1) * 32 + xj;
  const bool hasF = fid < 56;
  const int fks = (xg & 6) + (fid >= 28 ? 1 : 0), fcb = fid >= 28 ? fid - 28 : fid, fgrp = (xg >> 1) * 28 + (fid >> 1);
  const int rb = xg * 16 + (xj >> 1), ch = xj & 1;
  v4u dr[16], xa, xb;
  unsigned cpf = 0u;
  *(LAS unsigned*)(L + L_FLAG + lane * 4) = 0u;
  const unsigned long long tstart = __builtin_amdgcn_s_memrealtime();
  const bool isq = wave == 0;
  const int ks = xg >> 1, cb = 2 * xj + ((xg ^ xj) & 1);
  const unsigned wlo = (((unsigned)(ab * 8 + xg) << 23) | 0x700000u);
  const int wofs = (4 * as + w) * 1024;
  const int va = ((c8 * (isq ? 16 : 0x11000)) ^ (isq ? 0 : wofs)) + (r0 << (isq ? 17 : 6)) + (a4 << (isq ? 18 : 4));
  if (isq || role == 0) {
    const int sstr = isq ? 8192 : 128;
    int so[8];
    so[0] = isq ? (ks << 20) + cb * 128 : 0;
#pragma unroll
    for (int u = 1; u < 8; ++u) { so[u] = so[u - 1] + sstr; asm volatile("" : "+s"(so[u])); }
    const rsrc_t r1 = mkrs(isq ? p.wq : p.k + wlo, 512 * 8192);
    asm volatile("s_waitcnt lgkmcnt(0)" :: "v"(va) : "memory");
    SB();
    if (isq) {
      const int xo = (abb * 512 + 128 * ks + 16 * rho) * 2;
      xa = *(const GAS v4u*)(p.x + xo);
      xb = *(const GAS v4u*)(p.x + xo + 16);
    } else {
      __builtin_amdgcn_s_barrier();
    }
    SB();
#pragma unroll
    for (int u = 0; u < 8; ++u) { dr[u] = ldnt(r1, va, so[u]); SB(); }
    const int vb = va ^ (isq ? 65536 : 0x88040);
    if (!isq) lds_wait1<1>(L, F_QP);
#pragma unroll
    for (int u = 0; u < 8; ++u) { dr[8 + u] = ldnt(r1, vb, so[u]); SB(); }
    if (!isq) lds_set(L, F_KISS + w);
    if (isq) {
      __builtin_amdgcn_s_barrier();
      SB();
      __builtin_amdgcn_s_setprio(3);
      const unsigned ep = *(const __attribute__((address_space(4))) unsigned*)(unsigned long long)p.ws;
      SB();
      unsigned e0, e1, o0, o1;
      delta_sel(n16 & 3, 0u, e0, e1);
      delta_sel(n16 & 3, 2u, o0, o1);
      const int off = WS_Q + ((((cb >> 3) * 2 + (a4 & 1)) * 4 + ks) * 128 + (cb & 7) * 16 + (a4 >> 1) * 8 + n16) * 16;
      f32x4 d0 = {0.f, 0.f, 0.f, 0.f}, d1 = {0.f, 0.f, 0.f, 0.f};
#pragma unroll
      for (int u = 0; u < 16; ++u) {
        const unsigned xs = (u < 8) ? xa[(u >> 1) & 3] : xb[(u >> 1) & 3];
        const unsigned A0 = PERM(xs, (u & 1) ? o0 : e0), A1 = PERM(xs, (u & 1) ? o1 : e1);
        const v4u wv = dr[u];
        d0 = mfma16(A0, A1, wv.x, wv.y, d0);
        d1 = mfma16(A0, A1, wv.z, wv.w, d1);
      }
      float sv[4];
#pragma unroll
      for (int i = 0; i < 4; ++i) sv[i] = swap32_add(dppf_rows<ROR8, 0xc>(d0[i]), dppf_rows<ROR8, 0xc>(d1[i]));
      stu(mkrs(p.ws, WS_RSRC_BYTES), n16 < 8 ? off : OOB, pkbf(sv[0], sv[1]), pkbf(sv[2], sv[3]), (ep + 256u) & ~255u);
      SB();
      lds_set(L, F_QP);
      __builtin_amdgcn_s_setprio(0);
    }
  } else {
    if (wave == 1) {
      unsigned long long pc0;
      int code_bytes;
      asm volatile("s_getpc_b64 %0\n.Lfk_pc:\n\ts_mov_b32 %1, .Lfk_end-.Lfk_pc" : "=s"(pc0), "=s"(code_bytes));
      cpf = __builtin_amdgcn_raw_buffer_load_b32(mkrs((const unsigned char*)pc0, code_bytes), (lane * 4 + (xj & 3)) * 128, 0, 0);
    }
    asm volatile("s_waitcnt lgkmcnt(0)" ::: "memory");
    __builtin_amdgcn_s_barrier();
  }
  SB();
  const unsigned epoch = *(const __attribute__((address_space(4))) unsigned*)(unsigned long long)p.ws;
  SB();
  unsigned sE0, sE1, sO0, sO1;
  delta_sel(n16 & 3, 0u, sE0, sE1);
  delta_sel(n16 & 3, 2u, sO0, sO1);
  SB();
  const rsrc_t rs = mkrs(p.ws, WS_RSRC_BYTES);
  unsigned tag = (epoch + 256u) & ~255u;
  asm volatile("" : "+s"(tag));

  if (role == 2) {
    {
      const rsrc_t grs_ = mkrs(p.x, w == 1 ? 2048 : 0), gwrs = mkrs(p.gw, w == 1 ? 1024 : 0);
      const v4u ga = __builtin_amdgcn_raw_buffer_load_b128(grs_, lane * 16, 0, 0);
      const v4u gb = __builtin_amdgcn_raw_buffer_load_b128(grs_, lane * 16 + 1024, 0, 0);
      const v4u gw = __builtin_amdgcn_raw_buffer_load_b128(gwrs, lane * 16, 0, 0);
      const unsigned wv = __builtin_amdgcn_raw_buffer_load_b32(rs, (w == 2 && lane < 46) ? WS_F + (blk * 46 + lane) * 128 : OOB, 0, SC1);
      SB();
      lds_set(L, F_FT + w);
      SB();
      asm volatile("" :: "v"(wv), "v"(ga), "v"(gb), "v"(gw), "v"(dr[15]));
      if (w == 1) {
        float s0 = 0.f, s1 = 0.f;
#pragma unroll
        for (int i = 0; i < 4; ++i) {
          s0 = fmaf(bflo(ga[i]), bflo(gw[i]), s0); s0 = fmaf(bfhi(ga[i]), bfhi(gw[i]), s0);
          s1 = fmaf(bflo(gb[i]), bflo(gw[i]), s1); s1 = fmaf(bfhi(gb[i]), bfhi(gw[i]), s1);
        }
        float sg = swap32_add(s0, s1);
        sg = row_sum(swap16_add(sg, sg));
        const float gt = 1.f / (1.f + __expf(-sg));
        if ((lane & 31) == 0) *(LAS float*)(L + L_GT + (lane >> 5) * 4) = gt;
        lds_set(L, F_G);
      }
    }
    const rsrc_t drs = mkrs(p.wd, hasF ? 512 * 3584 : 0), ors = mkrs(p.wo, 4096 * 1024);
    const int co = ch * 512 + w * 128 + c8 * 16;
    const int dso = ((fgrp >> 2) * 64 + (fgrp & 1) * 32 + ((fgrp >> 1) & 1) * 4) * 1024;
    const int dvo = ((rho >> 1) * 8 + (rho & 1) * 2) * 1024 + co;
    int roff = WS_R + ((((ch * 4 + w) * 4 + a4) * 2 + (c8 >> 2)) * 128 + rb) * 64 + (c8 & 3) * 16;
    roff = n16 < 8 ? roff : OOB;
    asm volatile("" : "+v"(roff));
    SB();
    lds_wait4<1>(L, F_VISS);
    v4u wdr[2], wor[4];
#pragma unroll
    for (int u = 0; u < 2; ++u) { wdr[u] = ldnt(drs, dvo, dso + u * 1024); SB(); }
#pragma unroll
    for (int u = 0; u < 4; ++u) { wor[u] = ldnt(ors, rho * 4096 + co, rb * 32768 + u * 1024); SB(); }
    asm volatile("" :: "v"(wor[3]));
    v4u av;
    LDS_WAIT_WITH(F_AV, av = LDSV(v4u, L_AV + (abb * 8 + rho) * 16));
    __builtin_amdgcn_s_setprio(3);
    {
      f32x4 d0 = {0.f, 0.f, 0.f, 0.f}, d1 = {0.f, 0.f, 0.f, 0.f}, d2 = {0.f, 0.f, 0.f, 0.f}, d3 = {0.f, 0.f, 0.f, 0.f};
#pragma unroll
      for (int u = 0; u < 6; ++u) {
        const unsigned xs = av[u >> 1];
        const unsigned A0 = PERM(xs, (u & 1) ? sO0 : sE0), A1 = PERM(xs, (u & 1) ? sO1 : sE1);
        const v4u wv = u < 4 ? wor[u & 3] : wdr[u & 1];
        if (u & 1) { d2 = mfma16(A0, A1, wv.x, wv.y, d2); d3 = mfma16(A0, A1, wv.z, wv.w, d3); }
        else { d0 = mfma16(A0, A1, wv.x, wv.y, d0); d1 = mfma16(A0, A1, wv.z, wv.w, d1); }
      }
      d0 += d2; d1 += d3;
      float sv[4];
#pragma unroll
      for (int i = 0; i < 4; ++i) sv[i] = swap32_add(dppf_rows<ROR8, 0xc>(d0[i]), dppf_rows<ROR8, 0xc>(d1[i]));
      stu(rs, roff, pkbf(sv[0], sv[1]), pkbf(sv[2], sv[3]), tag);
    }
    SB();
    lds_set(L, F_PUB + w);
  } else if (role == 1) {
    __builtin_amdgcn_s_setprio(1);
    const int frow = 64 * fks + 32 * (w >> 1) + 4 * rho;
    int ooff = lane < 32 ? WS_O + (((xg * 16 + w * 4 + (rho & 3)) * 32 + xj) * 8 + c8) * 16 : OOB;
    const v2u xf = *(const GAS v2u*)(p.x + (abb * 512 + frow) * 2);
    v4u vr[16], gu[4];
    {
      const rsrc_t vrs = mkrs(p.v + wlo, 1 << 20);
      lds_wait4<1>(L, F_KISS);
#pragma unroll
      for (int u = 0; u < 16; ++u) { vr[u] = ldnt(vrs, lane * 16, wofs ^ (u * 0x11000)); SB(); }
      lds_set(L, F_GU + w);
    }
    {
      const rsrc_t grs = mkrs((w & 1) ? p.wu : p.wg, hasF ? 512 * 3584 : 0);
      const int go = frow * 3584 + fcb * 128 + c8 * 16;
      lds_wait4<1>(L, F_GU);
#pragma unroll
      for (int u = 0; u < 4; ++u) { gu[u] = ldnt(grs, go, u * 3584); SB(); }
    }
    lds_set(L, F_VISS + w);
    int foff = WS_F + (fcb * 4 + (a4 >> 1) * 2 + (c8 >> 2)) * 4096 + ((((a4 & 1) * 2 + (w & 1)) * 16 + fks * 2 + (w >> 1)) * 4 + (c8 & 3)) * 16;
    foff = (hasF && n16 < 8) ? foff : OOB;
    asm volatile("" : "+v"(ooff), "+v"(foff));
    lds_wait4<1>(L, F_FT);
    v4u pa, pb;
    {
      Spin sp;
      for (;;) {
        asm volatile("" ::: "memory");
        const unsigned f = LDSV(unsigned, L_FLAG + (F_P + w) * 4);
        pa = LDSV(v4u, L_P + w * 512 + (n16 >> 2) * 128 + a4 * 32); pb = LDSV(v4u, L_P + w * 512 + (n16 >> 2) * 128 + a4 * 32 + 16);
        if (__builtin_amdgcn_readfirstlane(f) != 0u) break;
        if (sp.expired()) break;
        __builtin_amdgcn_s_sleep(1);
      }
    }
    f32x4 o0 = {0.f, 0.f, 0.f, 0.f}, o1 = {0.f, 0.f, 0.f, 0.f}, o2 = {0.f, 0.f, 0.f, 0.f}, o3 = {0.f, 0.f, 0.f, 0.f};
    float fsc;
    unsigned otag0, otag1;
#pragma unroll
    for (int t = 0; t < 16; ++t) {
      const unsigned ps = (t < 8) ? pa[(t >> 1) & 3] : pb[(t >> 1) & 3];
      const unsigned A0 = PERM(ps, (t & 1) ? sO0 : sE0), A1 = PERM(ps, (t & 1) ? sO1 : sE1);
      if (t & 1) { o2 = mfma16(A0, A1, vr[t].x, vr[t].y, o2); o3 = mfma16(A0, A1, vr[t].z, vr[t].w, o3); }
      else { o0 = mfma16(A0, A1, vr[t].x, vr[t].y, o0); o1 = mfma16(A0, A1, vr[t].z, vr[t].w, o1); }
      if (t == 9) {
        f32x4 m, l;
        LDS_WAIT_WITH(F_L, m = LDSV(f32x4, L_MX + a4 * 16); l = LDSV(f32x4, L_LS + a4 * 16));
        const float M = vmax(vmax(m[0], m[1]), vmax(m[2], m[3]));
        const float e0 = __builtin_amdgcn_exp2f(m[0] - M), e1 = __builtin_amdgcn_exp2f(m[1] - M), e2 = __builtin_amdgcn_exp2f(m[2] - M), e3 = __builtin_amdgcn_exp2f(m[3] - M);
        const float ls = (e0 * l[0] + e1 * l[1]) + (e2 * l[2] + e3 * l[3]);
        fsc = (w == 0 ? e0 : (w == 1 ? e1 : (w == 2 ? e2 : e3))) * __builtin_amdgcn_rcpf(ls);
        const unsigned lse = __builtin_amdgcn_readlane(f2u(M + __builtin_amdgcn_logf(ls)), w * 16);
        const unsigned h16 = (lane & 1) ? lse : lse >> 16;
        otag0 = tag | (h16 & 255u); otag1 = tag | ((h16 >> 8) & 255u);
      }
    }
    *(LAS f32x4*)(L + L_OM + (w * 512 + a4 * 128 + n16 * 8) * 4) = (o0 + o2) * fsc;
    *(LAS f32x4*)(L + L_OM + (w * 512 + a4 * 128 + n16 * 8 + 4) * 4) = (o1 + o3) * fsc;
    lds_set(L, F_O + w);
    {
      const int ob = L_OM + ((lane >> 5) * 1024 + w * 128 + (lane & 31) * 4) * 4;
      f32x4 sa, sb;
      LDS_WAIT_WITH(F_O, sa = LDSV(f32x4, ob); sb = LDSV(f32x4, ob + 2048));
      const f32x4 s2 = sa + sb;
      const float t0 = swap32_add(s2[0], s2[2]), t1 = swap32_add(s2[1], s2[3]);
      const unsigned pk = pkbf(t0, t1);
      const auto sw = __builtin_amdgcn_permlane32_swap(pk, pk, false, false);
      const v4u u = {sw[0], otag0, sw[1], otag1};
      __builtin_amdgcn_raw_buffer_store_b128(u, rs, ooff, 0, SC1);
    }
    SB();
    {
      f32x4 d0 = {0.f, 0.f, 0.f, 0.f}, d1 = {0.f, 0.f, 0.f, 0.f};
#pragma unroll
      for (int u = 0; u < 4; ++u) {
        const unsigned xs = xf[u >> 1];
        const unsigned A0 = PERM(xs, (u & 1) ? sO0 : sE0), A1 = PERM(xs, (u & 1) ? sO1 : sE1);
        d0 = mfma16(A0, A1, gu[u].x, gu[u].y, d0);
        d1 = mfma16(A0, A1, gu[u].z, gu[u].w, d1);
      }
      float sv[4];
#pragma unroll
      for (int i = 0; i < 4; ++i) sv[i] = swap32_add(dppf_rows<ROR8, 0xc>(d0[i]), dppf_rows<ROR8, 0xc>(d1[i]));
      stu(rs, foff, pkbf(sv[0], sv[1]), pkbf(sv[2], sv[3]), tag);
    }
    SB();
    lds_set(L, F_FP + w);
    if (blk < 64) lds_wait4<1>(L, F_PUB);
  } else {
    __builtin_amdgcn_s_setprio(2);
    const int qoff = WS_Q + (((xg * 2 + ab) * 4 + 2 * (lane >> 5)) * 128 + w * 32 + (lane & 31)) * 16;
    const int pos = lane & 31;
    int qw = L_QS + (w * 128 + (pos >> 4) * 64 + (pos & 7) * 8 + ((pos >> 3) & 1) * 4 + (lane >> 5) * 2) * 2;
    const int qa = L_QS + ((n16 & 3) * 128 + ((n16 >> 2) & 1) * 32 + a4 * 8) * 2;
    int qa0o = n16 < 8 ? qa : L_ZERO, qa1o = n16 < 8 ? qa + 128 : L_ZERO, qc0o = n16 < 8 ? L_ZERO : qa, qc1o = n16 < 8 ? L_ZERO : qa + 128;
    asm volatile("" : "+v"(qw), "+v"(qa0o), "+v"(qa1o), "+v"(qc0o), "+v"(qc1o));
    wait_until(tstart + TQ);
    v4u q0 = ldu(rs, qoff), q1 = ldu(rs, qoff + 2048);
    SB();
    {
      SpinG sp;
#pragma unroll 1
      while (!__all(uok(q0, tag) && uok(q1, tag))) {
        asm volatile("" ::: "memory");
        REPOLL(q0, qoff); REPOLL(q1, qoff + 2048);
        if (sp.expired()) break;
      }
    }
    {
      constexpr float QS = 0.088388347648318440f * 1.4426950408889634f;
      const float c0 = bflo(q0.x) + bflo(q1.x), c1 = bfhi(q0.x) + bfhi(q1.x), c2 = bflo(q0.z) + bflo(q1.z), c3 = bfhi(q0.z) + bfhi(q1.z);
      const float e0 = swap32_add(c0, c2) * QS, e1 = swap32_add(c1, c3) * QS;
      *(LAS unsigned*)(L + qw) = pkbf(e0, e1);
    }
    lds_set(L, F_Q + w);
    v4u qa0, qa1, qc0, qc1;
    LDS_WAIT_WITH(F_Q, qa0 = LDSV(v4u, qa0o); qa1 = LDSV(v4u, qa1o); qc0 = LDSV(v4u, qc0o); qc1 = LDSV(v4u, qc1o));
    f32x4 acc[2][2];
#pragma unroll
    for (int e = 0; e < 2; ++e)
#pragma unroll
      for (int P = 0; P < 2; ++P) acc[e][P] = mfma32(qa0, dr[e * 8 + P * 2], f32x4{0.f, 0.f, 0.f, 0.f});
#pragma unroll
    for (int e = 0; e < 2; ++e)
#pragma unroll
      for (int P = 0; P < 2; ++P) acc[e][P] = mfma32(qa1, dr[e * 8 + P * 2 + 1], acc[e][P]);
#pragma unroll
    for (int e = 0; e < 2; ++e)
#pragma unroll
      for (int P = 0; P < 2; ++P) acc[e][P] = mfma32(qc0, dr[e * 8 + P * 2 + 4], acc[e][P]);
#pragma unroll
    for (int e = 0; e < 2; ++e)
#pragma unroll
      for (int P = 0; P < 2; ++P) acc[e][P] = mfma32(qc1, dr[e * 8 + P * 2 + 5], acc[e][P]);
    const bool sel0 = ((a4 & 1) == (n16 >> 3));
    float S[4];
#pragma unroll
    for (int i = 0; i < 4; ++i) {
      const float y0 = dppf_rows<ROR8, 0xa>(sel0 ? acc[0][0][i] : acc[1][0][i]);
      const float y1 = dppf_rows<ROR8, 0xa>(sel0 ? acc[0][1][i] : acc[1][1][i]);
      S[i] = swap16_add(y0, y1);
    }
    const float mrow = row_max(swap16_max(swap32_max(S[0], S[1]), swap32_max(S[2], S[3])));
    const float m0 = rdlane(mrow, 0), m2 = rdlane(mrow, 16), m1 = rdlane(mrow, 32), m3 = rdlane(mrow, 48);
    const unsigned P01 = pkbf(__builtin_amdgcn_exp2f(S[0] - m0), __builtin_amdgcn_exp2f(S[1] - m1));
    const unsigned P23 = pkbf(__builtin_amdgcn_exp2f(S[2] - m2), __builtin_amdgcn_exp2f(S[3] - m3));
    {
      LAS unsigned short* pp = (LAS unsigned short*)(L + L_P + w * 512 + lane * 2);
      pp[0] = (unsigned short)P01; pp[64] = (unsigned short)(P01 >> 16); pp[128] = (unsigned short)P23; pp[192] = (unsigned short)(P23 >> 16);
      lds_set(L, F_P + w);
      SB();
      const float lrow = row_sum(swap16_add(swap32_add(bflo(P01), bfhi(P01)), swap32_add(bflo(P23), bfhi(P23))));
      const int hrow = ((a4 & 1) << 1) | (a4 >> 1);
      if (n16 == 0) { *(LAS float*)(L + L_MX + hrow * 16 + w * 4) = mrow; *(LAS float*)(L + L_LS + hrow * 16 + w * 4) = lrow; }
    }
    lds_set(L, F_L + w);
    const int bb = w >> 1;
    lds_wait1<1>(L, F_G);
    const float gate = *(LAS float*)(L + L_GT + bb * 4);
    const bool isO = (w & 1) == 0;
    int goff = isO ? WS_O + rb * 4096 + bb * 2048 + lane * 16 : (hasF ? WS_F + fgrp * 4096 + bb * 2048 + lane * 16 : OOB);
    int vslot = L_AV + (isO ? (bb * 8 + c8) * 16 + a4 * 2 : (bb * 8 + (lane & 3) * 2 + (a4 >> 1)) * 16 + (4 + (a4 & 1)) * 2);
    asm volatile("" : "+v"(goff), "+v"(vslot));
    lds_wait4<1>(L, F_FP);
    __builtin_amdgcn_s_sleep(TPD);
    v4u u0 = ldu(rs, goff), u1 = ldu(rs, goff + 1024);
    if (isO || hasF) {
      SpinG sp;
#pragma unroll 1
      while (!__all(uok(u0, tag) && uok(u1, tag))) {
        asm volatile("" ::: "memory");
        REPOLL(u0, goff); REPOLL(u1, goff + 1024);
        if (sp.expired()) break;
        __builtin_amdgcn_s_sleep(2);
      }
    }
    float val;
    if (isO) {
      const unsigned hm = __builtin_amdgcn_perm(__builtin_amdgcn_perm(u1.w, u1.y, 0x0c0c0400u), __builtin_amdgcn_perm(u0.w, u0.y, 0x0c0c0400u), 0x05040100u);
      const unsigned ho = (unsigned)__builtin_amdgcn_mov_dpp((int)hm, 0xB1, 0xf, 0xf, true);
      const unsigned hi = (lane & 1) ? ho : hm, lo = (lane & 1) ? hm : ho;
      const float l0 = u2f(__builtin_amdgcn_perm(hi, lo, 0x05040100u)), l1 = u2f(__builtin_amdgcn_perm(hi, lo, 0x07060302u));
      float mm = vmax(l0, l1);
      mm = vmax(mm, dppf<ROR8>(mm)); mm = xrow_max(mm);
      const float w0 = __builtin_amdgcn_exp2f(l0 - mm), w1 = __builtin_amdgcn_exp2f(l1 - mm);
      float ll = w0 + w1;
      ll += dppf<ROR8>(ll); ll = xrow_sum(ll);
      const float sc = gate * __builtin_amdgcn_rcpf(ll);
      const float v0 = w0 * bflo(u0.x) + w1 * bflo(u1.x), v1 = w0 * bfhi(u0.x) + w1 * bfhi(u1.x);
      const float v2 = w0 * bflo(u0.z) + w1 * bflo(u1.z), v3 = w0 * bfhi(u0.z) + w1 * bfhi(u1.z);
      val = swap16_add(swap32_add(v0, v2), swap32_add(v1, v3));
      val += dppf<ROR8>(val);
      val *= sc;
    } else {
      float gs = swap16_add(swap32_add(bflo(u0.x), bflo(u0.z)), swap32_add(bfhi(u0.x), bfhi(u0.z)));
      float us = swap16_add(swap32_add(bflo(u1.x), bflo(u1.z)), swap32_add(bfhi(u1.x), bfhi(u1.z)));
      gs += dppf<ROR8>(gs); us += dppf<ROR8>(us);
      gs += dppf<ROR4>(gs); us += dppf<ROR4>(us);
      val = gs * __builtin_amdgcn_rcpf(1.f + __expf(-gs)) * us * (1.f - gate);
    }
    *(LAS unsigned short*)(L + vslot) = (unsigned short)pkbf(val, val);
    lds_set(L, F_AV + w);
    if (blk < 64) lds_wait4<1>(L, F_PUB);
  }
  if (blk < 64) {
    if (role < 2) {
      const int roff = WS_R + blk * 8192 + (tid - 256) * 16;
      __builtin_amdgcn_s_sleep(TPD2);
      v4u u = ldu(rs, roff);
      SpinG sp;
#pragma unroll 1
      while (!__all(uok(u, tag))) {
        asm volatile("" ::: "memory");
        REPOLL(u, roff);
        if (sp.expired()) break;
        __builtin_amdgcn_s_sleep(2);
      }
      float vv = swap16_add(swap32_add(bflo(u.x), bflo(u.z)), swap32_add(bfhi(u.x), bfhi(u.z)));
      vv += dppf<ROR8>(vv); vv += dppf<ROR4>(vv);
      if ((lane & 12) == 0) *(LAS float*)(L + L_RED + (((lane & 3) * 4 + a4) * 8 + wave - 4) * 4) = vv;
    }
    __syncthreads();
    if (tid < 16) {
      const f32x4 s0 = *(LAS f32x4*)(L + L_RED + tid * 32), s1 = *(LAS f32x4*)(L + L_RED + tid * 32 + 16);
      const float sum = ((s0[0] + s0[1]) + (s0[2] + s0[3])) + ((s1[0] + s1[1]) + (s1[2] + s1[3]));
      const int col = (blk >> 3) * 64 + ((blk & 1) * 4 + (tid >> 2)) * 8 + ((blk >> 2) & 1) * 4 + (tid & 3), ob = (blk >> 1) & 1;
      *(GAS unsigned short*)(p.out + (ob * 512 + col) * 2) = (unsigned short)pkbf(sum, sum);
      if (blk == 0 && tid == 0) __hip_atomic_store((GAS unsigned*)(p.ws + WS_EPOCH), tag, RLX_AGENT);
    }
  }
  asm volatile(".Lfk_end:" :: "v"(cpf));
}

}

extern "C" void kernel_launch(void* const* d_in, const int* in_sizes, int n_in, void* d_out, int out_size, void* d_ws, size_t ws_size,
                              hipStream_t stream) {
  Params p;
  p.x = (const unsigned char*)d_in[0];
  p.k = (const unsigned char*)d_in[1];
  p.v = (const unsigned char*)d_in[2];
  p.wq = (const unsigned char*)d_in[3];
  p.wo = (const unsigned char*)d_in[4];
  p.wg = (const unsigned char*)d_in[6];
  p.wu = (const unsigned char*)d_in[7];
  p.wd = (const unsigned char*)d_in[8];
  p.gw = (const unsigned char*)d_in[9];
  p.out = (unsigned char*)d_out;
  p.ws = (unsigned char*)d_ws;
  hipLaunchKernelGGL(fused_kernel, dim3(256), dim3(768), 0, stream, p);
}
```

```cpp
#include <hip/hip_runtime.h>
#include <hip/hip_bf16.h>
#include <stdint.h>

#define GAS __attribute__((address_space(1)))
#define LAS __attribute__((address_space(3)))
#define RLX_AGENT __ATOMIC_RELAXED, __HIP_MEMORY_SCOPE_AGENT
#define SB() __builtin_amdgcn_sched_barrier(0)
typedef unsigned v4u __attribute__((ext_vector_type(4)));
typedef unsigned v2u __attribute__((ext_vector_type(2)));
typedef float f32x4 __attribute__((ext_vector_type(4)));
typedef __bf16 bf16x8 __attribute__((ext_vector_type(8)));
typedef __bf16 bf16x2 __attribute__((ext_vector_type(2)));
typedef short s16x4 __attribute__((ext_vector_type(4)));

namespace {

constexpr int WS_EPOCH = 0;
constexpr int WS_Q = 4096;
constexpr int WS_F = WS_Q + 8 * 2 * 4 * 128 * 16;
constexpr int WS_O = WS_F + 112 * 4096;
constexpr int WS_R = WS_O + 128 * 4096;
constexpr int WS_END = WS_R + 64 * 8192;
constexpr int WS_RSRC_BYTES = WS_END;
constexpr int OOB = 0x7fffff00;

constexpr int L_FLAG = 0;
constexpr int L_ZERO = 192;
constexpr int L_QS = 256;
constexpr int L_P = L_QS + 1024;
constexpr int L_MX = L_P + 2048;
constexpr int L_LS = L_MX + 64;
constexpr int L_AV = L_LS + 64;
constexpr int L_GT = L_AV + 256;
constexpr int L_RED = L_GT + 16;
constexpr int L_OM = L_RED + 512;
constexpr int L_BYTES = L_OM + 8192;
constexpr int F_KISS = 0, F_Q = 4, F_P = 8, F_L = 12, F_O = 16, F_AV = 20, F_PUB = 24, F_G = 28, F_QP = 29, F_FP = 32, F_VISS = 36, F_GU = 40, F_FT = 44;

struct Params {
  const unsigned char *x, *wq, *k, *v, *wo, *wg, *wu, *wd, *gw;
  unsigned char *out, *ws;
};

typedef __amdgpu_buffer_rsrc_t rsrc_t;
__device__ __forceinline__ rsrc_t mkrs(const unsigned char* base, int bytes) {
  return __builtin_amdgcn_make_buffer_rsrc((void*)base, 0, bytes, 0x00020000);
}
__device__ __forceinline__ v4u ldnt(rsrc_t r, int voff, int soff) { return __builtin_amdgcn_raw_buffer_load_b128(r, voff, soff, 2); }
__device__ __forceinline__ unsigned pkbf(float lo, float hi) {
  bf16x2 r = {(__bf16)lo, (__bf16)hi};
  return __builtin_bit_cast(unsigned, r);
}
__device__ __forceinline__ float bflo(unsigned u) { return __builtin_bit_cast(float, u << 16); }
__device__ __forceinline__ float bfhi(unsigned u) { return __builtin_bit_cast(float, u & 0xffff0000u); }
__device__ __forceinline__ float u2f(unsigned u) { return __builtin_bit_cast(float, u); }
__device__ __forceinline__ unsigned f2u(float f) { return __builtin_bit_cast(unsigned, f); }
__device__ __forceinline__ f32x4 mfma32(v4u a, v4u b, f32x4 c) {
  return __builtin_amdgcn_mfma_f32_16x16x32_bf16(__builtin_bit_cast(bf16x8, a), __builtin_bit_cast(bf16x8, b), c, 0, 0, 0);
}
__device__ __forceinline__ f32x4 mfma16(unsigned a0, unsigned a1, unsigned b0, unsigned b1, f32x4 c) {
  v2u a = {a0, a1}, b = {b0, b1};
  return __builtin_amdgcn_mfma_f32_16x16x16bf16_1k(__builtin_bit_cast(s16x4, a), __builtin_bit_cast(s16x4, b), c, 0, 0, 0);
}
template <int CTRL> __device__ __forceinline__ float dppf(float x) {
  return u2f(__builtin_amdgcn_mov_dpp((int)f2u(x), CTRL, 0xf, 0xf, true));
}
template <int CTRL, int ROWMASK> __device__ __forceinline__ float dppf_rows(float x) {
  return u2f(__builtin_amdgcn_update_dpp((int)f2u(x), (int)f2u(x), CTRL, ROWMASK, 0xf, false));
}
constexpr int ROR8 = 0x128, ROR4 = 0x124, ROR2 = 0x122, ROR1 = 0x121;
__device__ __forceinline__ float vmax(float a, float b) { return __builtin_amdgcn_fmed3f(a, b, 3.4028234e38f); }
__device__ __forceinline__ float swap32_add(float a, float b) {
  auto r = __builtin_amdgcn_permlane32_swap(f2u(a), f2u(b), false, false);
  return u2f(r[0]) + u2f(r[1]);
}
__device__ __forceinline__ float swap32_max(float a, float b) {
  auto r = __builtin_amdgcn_permlane32_swap(f2u(a), f2u(b), false, false);
  return vmax(u2f(r[0]), u2f(r[1]));
}
__device__ __forceinline__ float swap16_add(float a, float b) {
  auto r = __builtin_amdgcn_permlane16_swap(f2u(a), f2u(b), false, false);
  return u2f(r[0]) + u2f(r[1]);
}
__device__ __forceinline__ float swap16_max(float a, float b) {
  auto r = __builtin_amdgcn_permlane16_swap(f2u(a), f2u(b), false, false);
  return vmax(u2f(r[0]), u2f(r[1]));
}
__device__ __forceinline__ float xrow_max(float x) { x = swap16_max(x, x); return swap32_max(x, x); }
__device__ __forceinline__ float xrow_sum(float x) { x = swap16_add(x, x); return swap32_add(x, x); }
__device__ __forceinline__ float row_max(float x) {
  x = vmax(x, dppf<ROR8>(x)); x = vmax(x, dppf<ROR4>(x)); x = vmax(x, dppf<ROR2>(x)); return vmax(x, dppf<ROR1>(x));
}
__device__ __forceinline__ float row_sum(float x) {
  x += dppf<ROR8>(x); x += dppf<ROR4>(x); x += dppf<ROR2>(x); return x + dppf<ROR1>(x);
}
__device__ __forceinline__ float rdlane(float x, int l) { return u2f(__builtin_amdgcn_readlane(f2u(x), l)); }
constexpr int SC1 = 16;
__device__ __forceinline__ v4u ldu(rsrc_t rs, int off) { return __builtin_amdgcn_raw_buffer_load_b128(rs, off, 0, SC1); }
__device__ __forceinline__ void stu(rsrc_t rs, int off, unsigned v0, unsigned v1, unsigned tag) {
  v4u u = {v0, tag, v1, tag};
  __builtin_amdgcn_raw_buffer_store_b128(u, rs, off, 0, SC1);
}
__device__ __forceinline__ unsigned make_tag(unsigned epoch) { return (epoch * 0x9E3779B1u + 0x7F4A7C15u) & ~255u; }
template <unsigned LIM = 256u> __device__ __forceinline__ bool uok(v4u u, unsigned tag) { return ((u.y ^ tag) | (u.w ^ tag)) < LIM; }

template <int LOG2> struct SpinT {
  unsigned n = 0;
  __device__ __forceinline__ bool expired() { return ++n > (1u << LOG2); }
};
typedef SpinT<25> Spin;
typedef SpinT<21> SpinG;
template <int SLEEP> __device__ __forceinline__ void lds_wait4(LAS unsigned char* L, int grp) {
  Spin sp;
  for (;;) {
    const v4u f = *(volatile LAS v4u*)(L + L_FLAG + grp * 4);
    if (__builtin_amdgcn_readfirstlane(f.x & f.y & f.z & f.w) != 0u) break;
    if (sp.expired()) break;
    if (SLEEP) __builtin_amdgcn_s_sleep(SLEEP);
  }
  asm volatile("" ::: "memory");
}
template <int SLEEP> __device__ __forceinline__ void lds_wait1(LAS unsigned char* L, int word) {
  Spin sp;
  for (;;) {
    const unsigned f = *(volatile LAS unsigned*)(L + L_FLAG + word * 4);
    if (__builtin_amdgcn_readfirstlane(f) != 0u) break;
    if (sp.expired()) break;
    if (SLEEP) __builtin_amdgcn_s_sleep(SLEEP);
  }
  asm volatile("" ::: "memory");
}
__device__ __forceinline__ void lds_set(LAS unsigned char* L, int word) {
  asm volatile("" ::: "memory");
  *(volatile LAS unsigned*)(L + L_FLAG + word * 4) = 1u;
}
__device__ __forceinline__ void delta_sel(int jj, unsigned sb, unsigned& s0, unsigned& s1) {
  const unsigned pr = sb | ((sb + 1u) << 8);
  const unsigned lo = 0x0c0c0000u | pr, hi = (pr << 16) | 0x0c0cu, z = 0x0c0c0c0cu;
  s0 = jj == 0 ? lo : (jj == 1 ? hi : z);
  s1 = jj == 2 ? lo : (jj == 3 ? hi : z);
}
#define PERM(x, s) __builtin_amdgcn_perm(0u, (x), (s))

constexpr int TQ = 325;
constexpr int TPD = 12;
constexpr int TPD2 = 6;
__device__ __forceinline__ void wait_until(unsigned long long t) {
  while ((long long)(__builtin_amdgcn_s_memrealtime() - t) < 0) __builtin_amdgcn_s_sleep(1);
}
#define REPOLL(u, off, LIM) do { const bool ok_ = uok<LIM>(u, tag); const v4u n_ = ldu(rs, ok_ ? OOB : (off)); u = ok_ ? u : n_; } while (0)

#define LDS_WAIT_WITH(grp, BODY) do { Spin sp_; for (;;) { \
    asm volatile("" ::: "memory"); \
    const v4u f_ = *(volatile LAS v4u*)(L + L_FLAG + (grp) * 4); \
    BODY; \
    if (__builtin_amdgcn_readfirstlane(f_.x & f_.y & f_.z & f_.w) != 0u) break; \
    if (sp_.expired()) break; } } while (0)
#define LDSV(T, off) (*(volatile LAS T*)(L + (off)))

__global__ void __launch_bounds__(768, 3) fused_kernel(Params p) {
  __shared__ __attribute__((aligned(16))) unsigned char lds_raw[L_BYTES];
  LAS unsigned char* L = (LAS unsigned char*)lds_raw;
  const int tid = threadIdx.x, lane = tid & 63, wave = __builtin_amdgcn_readfirstlane(tid >> 6);
  const int blk = blockIdx.x, xg = blk & 7, xj = blk >> 3;
  const int n16 = lane & 15, a4 = lane >> 4, w = wave & 3;
  const int role = wave < 4 ? 2 : (wave >> 3);
  const int c8 = lane & 7, r0 = (lane >> 3) & 1, rho = lane >> 3;
  const int abb = (n16 >> 2) & 1;
  const int ab = xj >> 4, as = xj & 15;
  const bool hasF = xj < 28;
  const int fks = xg, fcb = xj, fgrp = xg * 14 + (xj >> 1);
  const int rb = xg * 16 + (xj >> 1), ch = xj & 1;
  v4u dr[16], xa, xb;
  *(LAS unsigned*)(L + L_FLAG + lane * 4) = 0u;
  const unsigned long long tstart = __builtin_amdgcn_s_memrealtime();
  const bool isq = wave == 0;
  const int ks = xg >> 1, cb = 2 * xj + ((xg ^ xj) & 1);
  const unsigned wlo = (((unsigned)(ab * 8 + xg) << 23) | 0x700000u);
  const int wofs = (4 * as + w) * 1024;
  const int va = ((c8 * (isq ? 16 : 0x11000)) ^ (isq ? 0 : wofs)) + (r0 << (isq ? 17 : 6)) + (a4 << (isq ? 18 : 4));
  if (isq || role == 0) {
    const int sstr = isq ? 8192 : 128;
    int so[8];
    so[0] = isq ? (ks << 20) + cb * 128 : 0;
#pragma unroll
    for (int u = 1; u < 8; ++u) { so[u] = so[u - 1] + sstr; asm volatile("" : "+s"(so[u])); }
    const rsrc_t r1 = mkrs(isq ? p.wq : p.k + wlo, 512 * 8192);
    asm volatile("s_waitcnt lgkmcnt(0)" :: "v"(va) : "memory");
    SB();
    if (isq) {
      const int xo = (abb * 512 + 128 * ks + 16 * rho) * 2;
      xa = *(const GAS v4u*)(p.x + xo);
      xb = *(const GAS v4u*)(p.x + xo + 16);
    } else {
      __builtin_amdgcn_s_barrier();
    }
    SB();
#pragma unroll
    for (int u = 0; u < 8; ++u) { dr[u] = ldnt(r1, va, so[u]); SB(); }
    const int vb = va ^ (isq ? 65536 : 0x88040);
    if (!isq) lds_wait1<1>(L, F_QP);
#pragma unroll
    for (int u = 0; u < 8; ++u) { dr[8 + u] = ldnt(r1, vb, so[u]); SB(); }
    if (!isq) lds_set(L, F_KISS + w);
    if (isq) {
      __builtin_amdgcn_s_barrier();
      SB();
      __builtin_amdgcn_s_setprio(3);
      const unsigned ep = *(const __attribute__((address_space(4))) unsigned*)(unsigned long long)p.ws;
      SB();
      unsigned e0, e1, o0, o1;
      delta_sel(n16 & 3, 0u, e0, e1);
      delta_sel(n16 & 3, 2u, o0, o1);
      const int off = WS_Q + ((((cb >> 3) * 2 + (a4 & 1)) * 4 + ks) * 128 + (cb & 7) * 16 + (a4 >> 1) * 8 + n16) * 16;
      f32x4 d0 = {0.f, 0.f, 0.f, 0.f}, d1 = {0.f, 0.f, 0.f, 0.f};
#pragma unroll
      for (int u = 0; u < 16; ++u) {
        const unsigned xs = (u < 8) ? xa[(u >> 1) & 3] : xb[(u >> 1) & 3];
        const unsigned A0 = PERM(xs, (u & 1) ? o0 : e0), A1 = PERM(xs, (u & 1) ? o1 : e1);
        const v4u wv = dr[u];
        d0 = mfma16(A0, A1, wv.x, wv.y, d0);
        d1 = mfma16(A0, A1, wv.z, wv.w, d1);
      }
      float sv[4];
#pragma unroll
      for (int i = 0; i < 4; ++i) sv[i] = swap32_add(dppf_rows<ROR8, 0xc>(d0[i]), dppf_rows<ROR8, 0xc>(d1[i]));
      const unsigned y0 = f2u(sv[0]) + 0x80u, y1 = f2u(sv[1]) + 0x80u, y2 = f2u(sv[2]) + 0x80u, y3 = f2u(sv[3]) + 0x80u, qt = make_tag(ep) & 0xffff0000u;
      const v4u qu = {__builtin_amdgcn_perm(y1, y0, 0x07060302u), qt | __builtin_amdgcn_perm(y1, y0, 0x0c0c0501u),
                      __builtin_amdgcn_perm(y3, y2, 0x07060302u), qt | __builtin_amdgcn_perm(y3, y2, 0x0c0c0501u)};
      __builtin_amdgcn_raw_buffer_store_b128(qu, mkrs(p.ws, WS_RSRC_BYTES), n16 < 8 ? off : OOB, 0, SC1);
      SB();
      lds_set(L, F_QP);
      __builtin_amdgcn_s_setprio(0);
    }
  } else {
    asm volatile("s_waitcnt lgkmcnt(0)" ::: "memory");
    __builtin_amdgcn_s_barrier();
  }
  SB();
  unsigned epoch = *(const __attribute__((address_space(4))) unsigned*)(unsigned long long)p.ws;
  SB();
  unsigned sE0, sE1, sO0, sO1;
  delta_sel(n16 & 3, 0u, sE0, sE1);
  delta_sel(n16 & 3, 2u, sO0, sO1);
  SB();
  const rsrc_t rs = mkrs(p.ws, WS_RSRC_BYTES);
  asm volatile("" : "+s"(epoch));
  const unsigned tag = make_tag(epoch);

  if (role == 2) {
    {
      const rsrc_t grs_ = mkrs(p.x, w == 1 ? 2048 : 0), gwrs = mkrs(p.gw, w == 1 ? 1024 : 0);
      const v4u ga = __builtin_amdgcn_raw_buffer_load_b128(grs_, lane * 16, 0, 0);
      const v4u gb = __builtin_amdgcn_raw_buffer_load_b128(grs_, lane * 16 + 1024, 0, 0);
      const v4u gw = __builtin_amdgcn_raw_buffer_load_b128(gwrs, lane * 16, 0, 0);
      const int wbase = w == 1 ? (blk < 64 ? WS_R + blk * 8192 : OOB) : (w == 2 ? WS_O + rb * 4096 : (hasF ? WS_F + fgrp * 4096 : OOB));
      const unsigned wv = __builtin_amdgcn_raw_buffer_load_b32(rs, (w != 0 && (w == 1 || lane < 32)) ? wbase + lane * 128 : OOB, 0, SC1);
      SB();
      lds_set(L, F_FT + w);
      SB();
      asm volatile("" :: "v"(wv), "v"(ga), "v"(gb), "v"(gw), "v"(dr[15]));
      if (w == 1) {
        float s0 = 0.f, s1 = 0.f;
#pragma unroll
        for (int i = 0; i < 4; ++i) {
          s0 = fmaf(bflo(ga[i]), bflo(gw[i]), s0); s0 = fmaf(bfhi(ga[i]), bfhi(gw[i]), s0);
          s1 = fmaf(bflo(gb[i]), bflo(gw[i]), s1); s1 = fmaf(bfhi(gb[i]), bfhi(gw[i]), s1);
        }
        float sg = swap32_add(s0, s1);
        sg = row_sum(swap16_add(sg, sg));
        const float gt = __builtin_amdgcn_rcpf(1.f + __expf(-sg));
        if ((lane & 30) == 0) *(LAS float*)(L + L_GT + (lane >> 5) * 8 + (lane & 1) * 4) = (lane & 1) ? 1.f - gt : gt;
        lds_set(L, F_G);
      }
    }
    const rsrc_t drs = mkrs(p.wd, hasF ? 512 * 3584 : 0), ors = mkrs(p.wo, 4096 * 1024);
    const int co = ch * 512 + w * 128 + c8 * 16;
    const int dso = ((fgrp >> 2) * 64 + (fgrp & 1) * 32 + ((fgrp >> 1) & 1) * 4) * 1024;
    const int dvo = ((rho >> 1) * 8 + (rho & 1) * 2) * 1024 + co;
    int roff = WS_R + ((((ch * 4 + w) * 4 + a4) * 2 + (c8 >> 2)) * 128 + rb) * 64 + (c8 & 3) * 16;
    roff = n16 < 8 ? roff : OOB;
    asm volatile("" : "+v"(roff));
    SB();
    lds_wait4<1>(L, F_VISS);
    v4u wdr[2], wor[4];
#pragma unroll
    for (int u = 0; u < 2; ++u) { wdr[u] = ldnt(drs, dvo, dso + u * 1024); SB(); }
#pragma unroll
    for (int u = 0; u < 4; ++u) { wor[u] = ldnt(ors, rho * 4096 + co, rb * 32768 + u * 1024); SB(); }
    asm volatile("" :: "v"(wor[3]));
    v4u av;
    LDS_WAIT_WITH(F_AV, av = LDSV(v4u, L_AV + (abb * 8 + rho) * 16));
    __builtin_amdgcn_s_setprio(3);
    {
      f32x4 d0 = {0.f, 0.f, 0.f, 0.f}, d1 = {0.f, 0.f, 0.f, 0.f}, d2 = {0.f, 0.f, 0.f, 0.f}, d3 = {0.f, 0.f, 0.f, 0.f};
#pragma unroll
      for (int u = 0; u < 6; ++u) {
        const unsigned xs = av[u >> 1];
        const unsigned A0 = PERM(xs, (u & 1) ? sO0 : sE0), A1 = PERM(xs, (u & 1) ? sO1 : sE1);
        const v4u wv = u < 4 ? wor[u & 3] : wdr[u & 1];
        if (u & 1) { d2 = mfma16(A0, A1, wv.x, wv.y, d2); d3 = mfma16(A0, A1, wv.z, wv.w, d3); }
        else { d0 = mfma16(A0, A1, wv.x, wv.y, d0); d1 = mfma16(A0, A1, wv.z, wv.w, d1); }
      }
      d0 += d2; d1 += d3;
      float sv[4];
#pragma unroll
      for (int i = 0; i < 4; ++i) sv[i] = swap32_add(dppf_rows<ROR8, 0xc>(d0[i]), dppf_rows<ROR8, 0xc>(d1[i]));
      stu(rs, roff, pkbf(sv[0], sv[1]), pkbf(sv[2], sv[3]), tag);
    }
    SB();
    lds_set(L, F_PUB + w);
  } else if (role == 1) {
    __builtin_amdgcn_s_setprio(1);
    const int frow = 64 * fks + 32 * (w >> 1) + 4 * rho;
    int ooff = lane < 32 ? WS_O + (((xg * 16 + w * 4 + (rho & 3)) * 32 + xj) * 8 + c8) * 16 : OOB;
    const v2u xf = *(const GAS v2u*)(p.x + (abb * 512 + frow) * 2);
    v4u vr[16], gu[4];
    {
      const rsrc_t vrs = mkrs(p.v + wlo, 1 << 20);
      lds_wait4<1>(L, F_KISS);
#pragma unroll
      for (int u = 0; u < 16; ++u) { vr[u] = ldnt(vrs, lane * 16, wofs ^ (u * 0x11000)); SB(); }
      lds_set(L, F_GU + w);
    }
    {
      const rsrc_t grs = mkrs((w & 1) ? p.wu : p.wg, hasF ? 512 * 3584 : 0);
      const int go = frow * 3584 + fcb * 128 + c8 * 16;
      lds_wait4<1>(L, F_GU);
#pragma unroll
      for (int u = 0; u < 4; ++u) { gu[u] = ldnt(grs, go, u * 3584); SB(); }
    }
    lds_set(L, F_VISS + w);
    int foff = WS_F + (fcb * 4 + (a4 >> 1) * 2 + (c8 >> 2)) * 4096 + ((((a4 & 1) * 2 + (w & 1)) * 16 + fks * 2 + (w >> 1)) * 4 + (c8 & 3)) * 16;
    foff = (hasF && n16 < 8) ? foff : OOB;
    asm volatile("" : "+v"(ooff), "+v"(foff));
    lds_wait4<1>(L, F_FT);
    v4u pa, pb;
    {
      Spin sp;
      for (;;) {
        asm volatile("" ::: "memory");
        const unsigned f = LDSV(unsigned, L_FLAG + (F_P + w) * 4);
        pa = LDSV(v4u, L_P + w * 512 + (n16 >> 2) * 128 + a4 * 32); pb = LDSV(v4u, L_P + w * 512 + (n16 >> 2) * 128 + a4 * 32 + 16);
        if (__builtin_amdgcn_readfirstlane(f) != 0u) break;
        if (sp.expired()) break;
        __builtin_amdgcn_s_sleep(1);
      }
    }
    f32x4 o0 = {0.f, 0.f, 0.f, 0.f}, o1 = {0.f, 0.f, 0.f, 0.f}, o2 = {0.f, 0.f, 0.f, 0.f}, o3 = {0.f, 0.f, 0.f, 0.f};
    float fsc;
    unsigned otag0, otag1;
#pragma unroll
    for (int t = 0; t < 16; ++t) {
      const unsigned ps = (t < 8) ? pa[(t >> 1) & 3] : pb[(t >> 1) & 3];
      const unsigned A0 = PERM(ps, (t & 1) ? sO0 : sE0), A1 = PERM(ps, (t & 1) ? sO1 : sE1);
      if (t & 1) { o2 = mfma16(A0, A1, vr[t].x, vr[t].y, o2); o3 = mfma16(A0, A1, vr[t].z, vr[t].w, o3); }
      else { o0 = mfma16(A0, A1, vr[t].x, vr[t].y, o0); o1 = mfma16(A0, A1, vr[t].z, vr[t].w, o1); }
      if (t == 9) {
        f32x4 m, l;
        LDS_WAIT_WITH(F_L, m = LDSV(f32x4, L_MX + a4 * 16); l = LDSV(f32x4, L_LS + a4 * 16));
        const float M = vmax(vmax(m[0], m[1]), vmax(m[2], m[3]));
        const float e0 = __builtin_amdgcn_exp2f(m[0] - M), e1 = __builtin_amdgcn_exp2f(m[1] - M), e2 = __builtin_amdgcn_exp2f(m[2] - M), e3 = __builtin_amdgcn_exp2f(m[3] - M);
        const float ls = (e0 * l[0] + e1 * l[1]) + (e2 * l[2] + e3 * l[3]);
        fsc = (w == 0 ? e0 : (w == 1 ? e1 : (w == 2 ? e2 : e3))) * __builtin_amdgcn_rcpf(ls);
        const unsigned lse = __builtin_amdgcn_readlane(f2u(M + __builtin_amdgcn_logf(ls)), w * 16);
        const unsigned h16 = (lane & 1) ? lse : lse >> 16;
        otag0 = tag | (h16 & 255u); otag1 = tag | ((h16 >> 8) & 255u);
      }
    }
    *(LAS f32x4*)(L + L_OM + (w * 512 + a4 * 128 + n16 * 8) * 4) = (o0 + o2) * fsc;
    *(LAS f32x4*)(L + L_OM + (w * 512 + a4 * 128 + n16 * 8 + 4) * 4) = (o1 + o3) * fsc;
    lds_set(L, F_O + w);
    {
      const int ob = L_OM + ((lane >> 5) * 1024 + w * 128 + (lane & 31) * 4) * 4;
      f32x4 sa, sb;
      LDS_WAIT_WITH(F_O, sa = LDSV(f32x4, ob); sb = LDSV(f32x4, ob + 2048));
      const f32x4 s2 = sa + sb;
      const float t0 = swap32_add(s2[0], s2[2]), t1 = swap32_add(s2[1], s2[3]);
      const unsigned pk = pkbf(t0, t1);
      const auto sw = __builtin_amdgcn_permlane32_swap(pk, pk, false, false);
      const v4u u = {sw[0], otag0, sw[1], otag1};
      __builtin_amdgcn_raw_buffer_store_b128(u, rs, ooff, 0, SC1);
    }
    SB();
    {
      f32x4 d0 = {0.f, 0.f, 0.f, 0.f}, d1 = {0.f, 0.f, 0.f, 0.f};
#pragma unroll
      for (int u = 0; u < 4; ++u) {
        const unsigned xs = xf[u >> 1];
        const unsigned A0 = PERM(xs, (u & 1) ? sO0 : sE0), A1 = PERM(xs, (u & 1) ? sO1 : sE1);
        d0 = mfma16(A0, A1, gu[u].x, gu[u].y, d0);
        d1 = mfma16(A0, A1, gu[u].z, gu[u].w, d1);
      }
      float sv[4];
#pragma unroll
      for (int i = 0; i < 4; ++i) sv[i] = swap32_add(dppf_rows<ROR8, 0xc>(d0[i]), dppf_rows<ROR8, 0xc>(d1[i]));
      stu(rs, foff, pkbf(sv[0], sv[1]), pkbf(sv[2], sv[3]), tag);
    }
    SB();
    lds_set(L, F_FP + w);
    if (blk < 64) lds_wait4<1>(L, F_PUB);
  } else {
    __builtin_amdgcn_s_setprio(2);
    const int qoff = WS_Q + (((xg * 2 + ab) * 4 + 2 * (lane >> 5)) * 128 + w * 32 + (lane & 31)) * 16;
    const int pos = lane & 31;
    int qw = L_QS + (w * 128 + (pos >> 4) * 64 + (pos & 7) * 8 + ((pos >> 3) & 1) * 4 + (lane >> 5) * 2) * 2;
    const int qa = L_QS + ((n16 & 3) * 128 + ((n16 >> 2) & 1) * 32 + a4 * 8) * 2;
    int qa0o = n16 < 8 ? qa : L_ZERO, qa1o = n16 < 8 ? qa + 128 : L_ZERO, qc0o = n16 < 8 ? L_ZERO : qa, qc1o = n16 < 8 ? L_ZERO : qa + 128;
    asm volatile("" : "+v"(qw), "+v"(qa0o), "+v"(qa1o), "+v"(qc0o), "+v"(qc1o));
    wait_until(tstart + TQ);
    v4u q0 = ldu(rs, qoff), q1 = ldu(rs, qoff + 2048);
    SB();
    {
      SpinG sp;
#pragma unroll 1
      while (!__all(uok<65536u>(q0, tag) && uok<65536u>(q1, tag))) {
        asm volatile("" ::: "memory");
        REPOLL(q0, qoff, 65536u); REPOLL(q1, qoff + 2048, 65536u);
        if (sp.expired()) break;
      }
    }
    {
#define QEV(v, t) u2f(__builtin_amdgcn_perm((v), (t), 0x0504000cu))
#define QOD(v, t) u2f(__builtin_amdgcn_perm((v), (t), 0x0706010cu))
      const float c0 = QEV(q0.x, q0.y) + QEV(q1.x, q1.y), c1 = QOD(q0.x, q0.y) + QOD(q1.x, q1.y);
      const float c2 = QEV(q0.z, q0.w) + QEV(q1.z, q1.w), c3 = QOD(q0.z, q0.w) + QOD(q1.z, q1.w);
#undef QEV
#undef QOD
      const float e0 = swap32_add(c0, c2), e1 = swap32_add(c1, c3);
      *(LAS unsigned*)(L + qw) = pkbf(e0, e1);
    }
    lds_set(L, F_Q + w);
    v4u qa0, qa1, qc0, qc1;
    LDS_WAIT_WITH(F_Q, qa0 = LDSV(v4u, qa0o); qa1 = LDSV(v4u, qa1o); qc0 = LDSV(v4u, qc0o); qc1 = LDSV(v4u, qc1o));
    f32x4 acc[2][2];
#pragma unroll
    for (int e = 0; e < 2; ++e)
#pragma unroll
      for (int P = 0; P < 2; ++P) acc[e][P] = mfma32(qa0, dr[e * 8 + P * 2], f32x4{0.f, 0.f, 0.f, 0.f});
#pragma unroll
    for (int e = 0; e < 2; ++e)
#pragma unroll
      for (int P = 0; P < 2; ++P) acc[e][P] = mfma32(qa1, dr[e * 8 + P * 2 + 1], acc[e][P]);
#pragma unroll
    for (int e = 0; e < 2; ++e)
#pragma unroll
      for (int P = 0; P < 2; ++P) acc[e][P] = mfma32(qc0, dr[e * 8 + P * 2 + 4], acc[e][P]);
#pragma unroll
    for (int e = 0; e < 2; ++e)
#pragma unroll
      for (int P = 0; P < 2; ++P) acc[e][P] = mfma32(qc1, dr[e * 8 + P * 2 + 5], acc[e][P]);
    const bool sel0 = ((a4 & 1) == (n16 >> 3));
    float S[4];
#pragma unroll
    for (int i = 0; i < 4; ++i) {
      const float y0 = dppf_rows<ROR8, 0xa>(sel0 ? acc[0][0][i] : acc[1][0][i]);
      const float y1 = dppf_rows<ROR8, 0xa>(sel0 ? acc[0][1][i] : acc[1][1][i]);
      S[i] = swap16_add(y0, y1);
    }
    {
      constexpr float SCALE = 0.088388347648318440f;
      const unsigned r01 = pkbf(S[0], S[1]), r23 = pkbf(S[2], S[3]);
      const unsigned t01 = pkbf(bflo(r01) * SCALE, bfhi(r01) * SCALE), t23 = pkbf(bflo(r23) * SCALE, bfhi(r23) * SCALE);
      S[0] = bflo(t01); S[1] = bfhi(t01); S[2] = bflo(t23); S[3] = bfhi(t23);
    }
    constexpr float LOG2E = 1.4426950408889634f;
    const float mrow = row_max(swap16_max(swap32_max(S[0], S[1]), swap32_max(S[2], S[3]))) * LOG2E;
    const float m0 = rdlane(mrow, 0), m2 = rdlane(mrow, 16), m1 = rdlane(mrow, 32), m3 = rdlane(mrow, 48);
    const unsigned P01 = pkbf(__builtin_amdgcn_exp2f(fmaf(S[0], LOG2E, -m0)), __builtin_amdgcn_exp2f(fmaf(S[1], LOG2E, -m1)));
    const unsigned P23 = pkbf(__builtin_amdgcn_exp2f(fmaf(S[2], LOG2E, -m2)), __builtin_amdgcn_exp2f(fmaf(S[3], LOG2E, -m3)));
    {
      LAS unsigned short* pp = (LAS unsigned short*)(L + L_P + w * 512 + lane * 2);
      pp[0] = (unsigned short)P01; pp[64] = (unsigned short)(P01 >> 16); pp[128] = (unsigned short)P23; pp[192] = (unsigned short)(P23 >> 16);
      lds_set(L, F_P + w);
      SB();
      const float lrow = row_sum(swap16_add(swap32_add(bflo(P01), bfhi(P01)), swap32_add(bflo(P23), bfhi(P23))));
      const int hrow = ((a4 & 1) << 1) | (a4 >> 1);
      if (n16 == 0) { *(LAS float*)(L + L_MX + hrow * 16 + w * 4) = mrow; *(LAS float*)(L + L_LS + hrow * 16 + w * 4) = lrow; }
    }
    lds_set(L, F_L + w);
    const int bb = w >> 1;
    lds_wait1<1>(L, F_G);
    const float gate = *(LAS float*)(L + L_GT + w * 4);
    const bool isO = (w & 1) == 0;
    int goff = isO ? WS_O + rb * 4096 + bb * 2048 + lane * 16 : (hasF ? WS_F + fgrp * 4096 + bb * 2048 + lane * 16 : OOB);
    int vslot = L_AV + (isO ? (bb * 8 + c8) * 16 + a4 * 2 : (bb * 8 + (lane & 3) * 2 + (a4 >> 1)) * 16 + (4 + (a4 & 1)) * 2);
    asm volatile("" : "+v"(goff), "+v"(vslot));
    lds_wait4<1>(L, F_FP);
    __builtin_amdgcn_s_sleep(TPD);
    v4u u0 = ldu(rs, goff), u1 = ldu(rs, goff + 1024);
    if (isO || hasF) {
      SpinG sp;
#pragma unroll 1
      while (!__all(uok(u0, tag) && uok(u1, tag))) {
        asm volatile("" ::: "memory");
        REPOLL(u0, goff, 256u); REPOLL(u1, goff + 1024, 256u);
        if (sp.expired()) break;
        __builtin_amdgcn_s_sleep(2);
      }
    }
    float val;
    if (isO) {
      const unsigned hm = __builtin_amdgcn_perm(__builtin_amdgcn_perm(u1.w, u1.y, 0x0c0c0400u), __builtin_amdgcn_perm(u0.w, u0.y, 0x0c0c0400u), 0x05040100u);
      const unsigned ho = (unsigned)__builtin_amdgcn_mov_dpp((int)hm, 0xB1, 0xf, 0xf, true);
      const unsigned hi = (lane & 1) ? ho : hm, lo = (lane & 1) ? hm : ho;
      const float l0 = u2f(__builtin_amdgcn_perm(hi, lo, 0x05040100u)), l1 = u2f(__builtin_amdgcn_perm(hi, lo, 0x07060302u));
      float mm = vmax(l0, l1);
      mm = vmax(mm, dppf<ROR8>(mm)); mm = xrow_max(mm);
      const float w0 = __builtin_amdgcn_exp2f(l0 - mm), w1 = __builtin_amdgcn_exp2f(l1 - mm);
      float ll = w0 + w1;
      ll += dppf<ROR8>(ll); ll = xrow_sum(ll);
      const float sc = gate * __builtin_amdgcn_rcpf(ll);
      const float v0 = w0 * bflo(u0.x) + w1 * bflo(u1.x), v1 = w0 * bfhi(u0.x) + w1 * bfhi(u1.x);
      const float v2 = w0 * bflo(u0.z) + w1 * bflo(u1.z), v3 = w0 * bfhi(u0.z) + w1 * bfhi(u1.z);
      val = swap16_add(swap32_add(v0, v2), swap32_add(v1, v3));
      val += dppf<ROR8>(val);
      val *= sc;
    } else {
      float gs = swap16_add(swap32_add(bflo(u0.x), bflo(u0.z)), swap32_add(bfhi(u0.x), bfhi(u0.z)));
      float us = swap16_add(swap32_add(bflo(u1.x), bflo(u1.z)), swap32_add(bfhi(u1.x), bfhi(u1.z)));
      gs += dppf<ROR8>(gs); us += dppf<ROR8>(us);
      gs += dppf<ROR4>(gs); us += dppf<ROR4>(us);
      val = gs * __builtin_amdgcn_rcpf(1.f + __expf(-gs)) * us * gate;
    }
    *(LAS unsigned short*)(L + vslot) = (unsigned short)pkbf(val, val);
    lds_set(L, F_AV + w);
    if (blk < 64) lds_wait4<1>(L, F_PUB);
  }
  if (blk < 64) {
    if (role < 2) {
      const int roff = WS_R + blk * 8192 + (tid - 256) * 16;
      __builtin_amdgcn_s_sleep(TPD2);
      v4u u = ldu(rs, roff);
      SpinG sp;
#pragma unroll 1
      while (!__all(uok(u, tag))) {
        asm volatile("" ::: "memory");
        REPOLL(u, roff, 256u);
        if (sp.expired()) break;
        __builtin_amdgcn_s_sleep(2);
      }
      float vv = swap16_add(swap32_add(bflo(u.x), bflo(u.z)), swap32_add(bfhi(u.x), bfhi(u.z)));
      vv += dppf<ROR8>(vv); vv += dppf<ROR4>(vv);
      if ((lane & 12) == 0) *(LAS float*)(L + L_RED + (((lane & 3) * 4 + a4) * 8 + wave - 4) * 4) = vv;
    }
    __syncthreads();
    if (tid < 16) {
      const f32x4 s0 = *(LAS f32x4*)(L + L_RED + tid * 32), s1 = *(LAS f32x4*)(L + L_RED + tid * 32 + 16);
      const float sum = ((s0[0] + s0[1]) + (s0[2] + s0[3])) + ((s1[0] + s1[1]) + (s1[2] + s1[3]));
      const int col = (blk >> 3) * 64 + ((blk & 1) * 4 + (tid >> 2)) * 8 + ((blk >> 2) & 1) * 4 + (tid & 3), ob = (blk >> 1) & 1;
      *(GAS unsigned short*)(p.out + (ob * 512 + col) * 2) = (unsigned short)pkbf(sum, sum);
      if (blk == 0 && tid == 0) __hip_atomic_store((GAS unsigned*)(p.ws + WS_EPOCH), epoch + 1u, RLX_AGENT);
    }
  }
}

}

extern "C" void kernel_launch(void* const* d_in, const int* in_sizes, int n_in, void* d_out, int out_size, void* d_ws, size_t ws_size,
                              hipStream_t stream) {
  Params p;
  p.x = (const unsigned char*)d_in[0];
  p.k = (const unsigned char*)d_in[1];
  p.v = (const unsigned char*)d_in[2];
  p.wq = (const unsigned char*)d_in[3];
  p.wo = (const unsigned char*)d_in[4];
  p.wg = (const unsigned char*)d_in[6];
  p.wu = (const unsigned char*)d_in[7];
  p.wd = (const unsigned char*)d_in[8];
  p.gw = (const unsigned char*)d_in[9];
  p.out = (unsigned char*)d_out;
  p.ws = (unsigned char*)d_ws;
  hipLaunchKernelGGL(fused_kernel, dim3(256), dim3(768), 0, stream, p);
}
```
